```python
import torch

def _softmax(x, axis=-1):
    return torch.softmax(x, dim=axis)

def _gelu(x):
    return torch.nn.functional.gelu(x, approximate='tanh')

def _silu(x):
    return torch.nn.functional.silu(x)

def _sigmoid(x):
    return torch.sigmoid(x)

def _layernorm(x, axis=-1, eps=1e-05):
    m = x.mean(dim=axis, keepdim=True)
    v = x.var(dim=axis, keepdim=True, unbiased=False)
    return (x - m) / torch.sqrt(v + eps)

def _scatter_add(tgt, idx, src, axis=0):
    return tgt.clone().index_add_(axis, idx, src)

def _segment_sum(src, seg, n):
    out = torch.zeros((n,) + src.shape[1:], dtype=src.dtype)
    return out.index_add_(0, seg, src)

def _pad(x, pw):
    flat = tuple((v for pair in reversed(pw) for v in pair))
    return torch.nn.functional.pad(x, flat)
kernel_name = 'parallel_sum__memory_aug_recurrent_mem_tr__dense_activation_relu__prompt'

def setup_inputs():

    _w = lambda a: (lambda t: t.to(torch.bfloat16) if t.is_floating_point() else t)(torch.as_tensor(a))

    def _A_setup():
        B, S, Dm = (1, 2048, 512)
        H, Dh = (32, 128)
        M, seg = (8, 32)
        seg = min(seg, S)
        S = S // seg * seg
        return {'x': _w((lambda *s: torch.randn(*s, dtype=torch.bfloat16) * 0.05)(B, S, Dm)), 'mem0': _w((lambda *s: torch.randn(*s, dtype=torch.bfloat16) * 1.6)(B, M, Dm)), 'Wq': _w((lambda *s: torch.randn(*s, dtype=torch.bfloat16) * 0.8)(Dm, H, Dh) * Dm ** (-0.5)), 'Wk': _w((lambda *s: torch.randn(*s, dtype=torch.bfloat16) * 0.8)(Dm, H, Dh) * Dm ** (-0.5)), 'Wv': _w((lambda *s: torch.randn(*s, dtype=torch.bfloat16) * 1.6)(Dm, H, Dh) * Dm ** (-0.5)), 'Wo': _w((lambda *s: torch.randn(*s, dtype=torch.bfloat16) * 1.6)(H, Dh, Dm) * (H * Dh) ** (-0.5)), 'seg_len': seg}
    _A_in = _A_setup()

    def _B_setup():
        B, S, D, Dff = (1, 2048, 512, 1376)
        return {'x': _w((lambda *s: torch.randn(*s, dtype=torch.bfloat16) * 0.05)(B, S, D)), 'w1': _w((lambda *s: torch.randn(*s, dtype=torch.bfloat16) * 0.05)(D, Dff)), 'w2': _w((lambda *s: torch.randn(*s, dtype=torch.bfloat16) * 0.05)(Dff, D))}
    _B_in = _B_setup()
    return {'A_x': _A_in['x'], 'A_mem0': _A_in['mem0'], 'A_Wq': _A_in['Wq'], 'A_Wk': _A_in['Wk'], 'A_Wv': _A_in['Wv'], 'A_Wo': _A_in['Wo'], 'A_seg_len': _A_in['seg_len'], 'B_x': _B_in['x'], 'B_w1': _B_in['w1'], 'B_w2': _B_in['w2']}

def reference(A_x, A_mem0, A_Wq, A_Wk, A_Wv, A_Wo, A_seg_len, B_x, B_w1, B_w2):

    def _A_ref(x, mem0, Wq, Wk, Wv, Wo, seg_len):
        B, S, Dm = x.shape
        H, Dh = (Wq.shape[1], Wq.shape[2])
        M = mem0.shape[1]
        scale = Dh ** (-0.5)
        n_seg = S // seg_len
        L = M + seg_len
        idx = torch.arange(L)
        mask = (idx[:, None] < M) | (idx[:, None] >= idx[None, :])
        mem = mem0
        outs = []
        for s in range(n_seg):
            xs = x[:, s * seg_len:(s + 1) * seg_len, :].clone()
            h = torch.concatenate([mem, xs], axis=1)
            q = torch.einsum('btd,dhe->bhte', h, Wq)
            k = torch.einsum('btd,dhe->bhte', h, Wk)
            v = torch.einsum('btd,dhe->bhte', h, Wv)
            scores = torch.einsum('bhqe,bhke->bhqk', q, k) * scale
            scores = torch.where(mask[None, None], scores, -1000000000.0)
            p = _softmax(scores, axis=-1)
            o = torch.einsum('bhqk,bhke->bhqe', p, v)
            o = torch.einsum('bhte,hed->btd', o, Wo)
            mem = o[:, :M, :].clone()
            outs.append(o[:, M:, :])
        return torch.concatenate(outs, axis=1)

    def _B_ref(x, w1, w2):
        h = torch.einsum('bsd,df->bsf', x, w1)
        zero = torch.zeros_like(h)
        h = torch.maximum(h, zero)
        return torch.einsum('bsf,fd->bsd', h, w2)
    x = A_x
    xn = _layernorm(x, axis=-1)
    return x + _A_ref(xn, A_mem0, A_Wq, A_Wk, A_Wv, A_Wo, A_seg_len) + _B_ref(xn, B_w1, B_w2)
```

```cpp
#include <hip/hip_runtime.h>
#include <hip/hip_bf16.h>
#include <stdint.h>

typedef __bf16 bf16x2 __attribute__((ext_vector_type(2)));
typedef __bf16 bf16x8 __attribute__((ext_vector_type(8)));
typedef __bf16 bf16x16 __attribute__((ext_vector_type(16)));
typedef float f32x2 __attribute__((ext_vector_type(2)));
typedef float f32x4 __attribute__((ext_vector_type(4)));
typedef unsigned u32x2 __attribute__((ext_vector_type(2)));
typedef unsigned u32x4 __attribute__((ext_vector_type(4)));
typedef unsigned u32x8 __attribute__((ext_vector_type(8)));
typedef unsigned u32x32 __attribute__((ext_vector_type(32)));
typedef __amdgpu_buffer_rsrc_t rsrc_t;

#define DEV __device__ __forceinline__
#define SC1 16

constexpr int NSEG = 64;
constexpr unsigned MB = 1u << 20;
constexpr unsigned OFF_COMM = 0;
constexpr unsigned OFF_URING = 4 * MB;
constexpr unsigned OFF_CTRL = 6 * MB;
constexpr unsigned OFF_XN = 8 * MB;
constexpr unsigned OFF_BASE = 10 * MB;
constexpr unsigned OFF_P = 18 * MB;
constexpr unsigned OFF_KF = 24 * MB;
constexpr unsigned OFF_QF = 40 * MB;
constexpr unsigned OFF_U2 = 56 * MB;
constexpr unsigned OFF_VW = 72 * MB;
constexpr unsigned WS_END = 136 * MB;
#define C_EPOCH_ 0x600000
#define C_MSG_ 0x600100
#define C_XF_ 0x600200
constexpr unsigned C_VOTE = 0x600c00;
constexpr unsigned C_EPOCH = C_EPOCH_;
constexpr unsigned C_MSG = C_MSG_;
constexpr unsigned C_XF = C_XF_;
constexpr unsigned C_KQF = OFF_CTRL + 1024;
constexpr unsigned C_VWF = OFF_CTRL + 1536;
constexpr unsigned C_U2F = OFF_CTRL + 2048;
constexpr unsigned C_PF = OFF_CTRL + 2176;
constexpr unsigned C_BF = OFF_CTRL + 2304;
constexpr unsigned C_ERR = OFF_CTRL + 2560;
constexpr unsigned C_DBG = OFF_CTRL + 4096;
constexpr unsigned OOR = 0x40000000u;
static_assert(C_EPOCH == OFF_CTRL, "");

constexpr int SPIN_CAP = 1 << 17;
constexpr int LDS_BYTES = 128 * 1024;

#define DLY_QK_M 185
#define DLY_QK_K 50
#define DLY_VO_U 105
#define DLY_VO_P 125
#define DLY_VO_M 0

#define DLY_MP 0
#define DLY_PP 90

#define DLY_X 175
#define DLY_Q2 35

#define DLY_PRE 0
#define DLY_PRS 90
#define DLY_PACE 15
#define DLY_FFN 0

struct Params {
    const void *x, *mem0, *wq, *wk, *wv, *wo, *w1, *w2;
    void* out;
    unsigned char* ws;
    unsigned rolemask;
    unsigned dly[13];
};

DEV rsrc_t mkrs(const void* p, unsigned bytes) { return __builtin_amdgcn_make_buffer_rsrc((void*)p, 0, (int)bytes, 0x00020000); }
DEV u32x4 mksrd(const void* p, unsigned bytes) { return u32x4{(unsigned)(uintptr_t)p, (unsigned)((uintptr_t)p >> 32) & 0xffffu, bytes, 0x00020000u}; }
template <int AUX> DEV u32x4 ld128(rsrc_t r, unsigned v, unsigned s) { return __builtin_amdgcn_raw_buffer_load_b128(r, (int)v, (int)s, AUX); }
template <int AUX> DEV u32x2 ld64(rsrc_t r, unsigned v, unsigned s) { return __builtin_amdgcn_raw_buffer_load_b64(r, (int)v, (int)s, AUX); }
template <int AUX> DEV unsigned ld32(rsrc_t r, unsigned v, unsigned s) { return __builtin_amdgcn_raw_buffer_load_b32(r, (int)v, (int)s, AUX); }
template <int AUX> DEV void st128(u32x4 d, rsrc_t r, unsigned v, unsigned s) { __builtin_amdgcn_raw_buffer_store_b128(d, r, (int)v, (int)s, AUX); }
template <int AUX> DEV void st64(u32x2 d, rsrc_t r, unsigned v, unsigned s) { __builtin_amdgcn_raw_buffer_store_b64(d, r, (int)v, (int)s, AUX); }
template <int AUX> DEV void st32(unsigned d, rsrc_t r, unsigned v, unsigned s) { __builtin_amdgcn_raw_buffer_store_b32(d, r, (int)v, (int)s, AUX); }

DEV unsigned pk(float lo, float hi) { f32x2 v = {lo, hi}; return __builtin_bit_cast(unsigned, __builtin_convertvector(v, bf16x2)); }
DEV unsigned plo(unsigned a, unsigned b) { return __builtin_amdgcn_perm(b, a, 0x05040100u); }
DEV unsigned phi(unsigned a, unsigned b) { return __builtin_amdgcn_perm(b, a, 0x07060302u); }
DEV float asf(unsigned u) { return __builtin_bit_cast(float, u); }
DEV unsigned asu(float f) { return __builtin_bit_cast(unsigned, f); }
DEV f32x4 mfma(u32x4 a, u32x4 b, f32x4 c) {
    return __builtin_amdgcn_mfma_f32_16x16x32_bf16(__builtin_bit_cast(bf16x8, a), __builtin_bit_cast(bf16x8, b), c, 0, 0, 0);
}
DEV f32x4 smf(u32x4 a, u32x8 b, f32x4 c, int idx) {
    return __builtin_amdgcn_smfmac_f32_16x16x64_bf16(__builtin_bit_cast(bf16x8, a), __builtin_bit_cast(bf16x16, b), c, idx, 0, 0);
}
DEV u32x8 cat(u32x4 a, u32x4 b) { return __builtin_shufflevector(a, b, 0, 1, 2, 3, 4, 5, 6, 7); }
DEV float fold32(float a, float b) { auto r = __builtin_amdgcn_permlane32_swap(asu(a), asu(b), false, false); return asf(r[0]) + asf(r[1]); }
DEV float max32(float a) { auto r = __builtin_amdgcn_permlane32_swap(asu(a), asu(a), false, false); return fmaxf(asf(r[0]), asf(r[1])); }
DEV float max16(float a) { auto r = __builtin_amdgcn_permlane16_swap(asu(a), asu(a), false, false); return fmaxf(asf(r[0]), asf(r[1])); }
DEV float add16(float a) { auto r = __builtin_amdgcn_permlane16_swap(asu(a), asu(a), false, false); return asf(r[0]) + asf(r[1]); }
template <int CTRL> DEV float dppf(float a) { return asf(__builtin_amdgcn_update_dpp(0u, asu(a), CTRL, 0xf, 0xf, true)); }
DEV float rowmax(float a) { a = fmaxf(a, dppf<0x128>(a)); a = fmaxf(a, dppf<0x124>(a)); a = fmaxf(a, dppf<0x122>(a)); return fmaxf(a, dppf<0x121>(a)); }
DEV float rowsum(float a) { a += dppf<0x128>(a); a += dppf<0x124>(a); a += dppf<0x122>(a); return a + dppf<0x121>(a); }
DEV unsigned umax4(u32x4 a) { return max(max(a.x, a.y), max(a.z, a.w)); }
DEV unsigned nosent(unsigned a) { return min(a, 0xFFFFFFFEu); }

#define PIN_A(x) asm volatile("" : "+a"(x))
#define SCHED() __builtin_amdgcn_sched_barrier(0)
#define RFL(x) __builtin_amdgcn_readfirstlane(x)
#define WAIT_VM0() asm volatile("s_waitcnt vmcnt(0)" ::: "memory")
#define BAR() do { asm volatile("s_waitcnt lgkmcnt(0)" ::: "memory"); __builtin_amdgcn_s_barrier(); asm volatile("" ::: "memory"); } while (0)

DEV void delay(int n) {
    int t;
    asm volatile("s_mov_b32 %0, %1\n\t.p2align 6\n"
                 "1:\n\ts_sub_u32 %0, %0, 1\n\ts_cmp_gt_i32 %0, 0\n\ts_cbranch_scc1 1b" : "=&s"(t) : "s"(n) : "scc", "memory");
}

template <int NW> DEV unsigned wait_flags(rsrc_t rws, unsigned off, unsigned need, int& budget, int lane) {
    unsigned v;
    const unsigned a = off + (lane & (NW - 1)) * 4;
#pragma nounroll
    for (;;) {
        v = ld32<SC1>(rws, a, 0);
        if (__all(v > need) || --budget < 0) break;
        __builtin_amdgcn_s_sleep(4);
    }
    asm volatile("" ::: "memory");
    unsigned m = __builtin_amdgcn_readlane(v, 0);
#pragma unroll
    for (int i = 1; i < NW; ++i) m = min(m, (unsigned)__builtin_amdgcn_readlane(v, i));
    return m;
}

DEV void glds16(const unsigned char* g, char* l) {
    __builtin_amdgcn_global_load_lds((const __attribute__((address_space(1))) void*)g, (__attribute__((address_space(3))) void*)l, 16, 0, 0);
}

#define STAMP(slot)

constexpr float SC_LOG2 = 0.08838834764831845f * 1.4426950408889634f;

#define SMF "v_smfmac_f32_16x16x64_bf16 "
#define DPPM " row_mask:0xf bank_mask:0xf\n"
#define Z4(a, b, c, d) "v_mov_b32 v" #a ", 0\nv_mov_b32 v" #b ", 0\nv_mov_b32 v" #c ", 0\nv_mov_b32 v" #d ", 0\n"
#define DELAY(n, L) "s_mov_b32 s60, %[" n "]\n.p2align 6\n" L "%=:\ns_sub_u32 s60, s60, 1\ns_cmp_gt_i32 s60, 0\ns_cbranch_scc1 " L "%=\n"
#define PT(k)
#define PFLUSH
#define PDUMP(cu)
#define CL8(p) "v" #p "0", "v" #p "1", "v" #p "2", "v" #p "3", "v" #p "4", "v" #p "5", "v" #p "6", "v" #p "7", "v" #p "8", "v" #p "9"
#define AC8(p) "a" #p "0", "a" #p "1", "a" #p "2", "a" #p "3", "a" #p "4", "a" #p "5", "a" #p "6", "a" #p "7", "a" #p "8", "a" #p "9"
#define ACL AC8(), AC8(1), AC8(2), AC8(3), AC8(4), AC8(5), AC8(6), AC8(7), AC8(8), AC8(9), AC8(10), AC8(11), AC8(12), AC8(13), AC8(14), AC8(15), AC8(16), \
            AC8(17), AC8(18), AC8(19), AC8(20), AC8(21), AC8(22), AC8(23), AC8(24), "a250", "a251", "a252", "a253", "a254", "a255"
#define SCL "s60", "s61", "s62", "s63", "s64", "s65", "s66", "s67", "s68", "s69", "s70", "s71", "s72", "s73", "s74", "s75", "s76", "s77", "s78", "s79", \
            "s80", "s81", "s82", "s83", "s84", "s85", "s86", "s87", "s88", "s89", "s90", "s91", "s92", "s93", "s94", "s95"

#define Q4(A, b0, b1, b2, b3, X, Y) SMF "v[128:131], " A ", a[" b0 "], %[idx]\n" X SMF "v[132:135], " A ", a[" b1 "], %[idx]\n" \
                                    SMF "v[136:139], " A ", a[" b2 "], %[idx]\n" Y SMF "v[140:143], " A ", a[" b3 "], %[idx]\n"
#define QZERO Z4(128, 129, 130, 131) Z4(132, 133, 134, 135) Z4(136, 137, 138, 139) Z4(140, 141, 142, 143)
#define SZERO Z4(232, 233, 234, 235) Z4(236, 237, 238, 239) Z4(240, 241, 242, 243)
#define QK_WISSUE \
    "s_add_u32 s61, %[w], wl_t/2\ns_and_b32 s61, s61, 3\ns_lshl_b32 s61, s61, 20\n" \
    ".set wl_i, 0\n.rept 16\n" \
    "s_add_u32 s62, s61, ((wl_t&1)*4 + 2*(wl_i>>3) + 64*((wl_i>>1)&1) + 16*((wl_i>>2)&1) + 8*(wl_i&1))*8192\n" \
    "buffer_load_dwordx2 v[128+(wl_t&3)*32+wl_i*2:129+(wl_t&3)*32+wl_i*2], %[wvo], %[rw], s62 offen\n" \
    ".set wl_i, wl_i+1\n.endr\n.set wl_t, wl_t+1\n"
#define QK_WLOAD \
    "s_mov_b32 s63, 0x05040100\ns_mov_b32 s64, 0x07060302\n" \
    ".set wl_t, 0\n.rept 3\n" QK_WISSUE ".endr\n" \
    ".set wl_p, 0\n.rept 8\n" \
    ".if wl_p < 6\ns_waitcnt vmcnt(32)\n.elseif wl_p == 6\ns_waitcnt vmcnt(16)\n.else\ns_waitcnt vmcnt(0)\n.endif\n" \
    ".if wl_t < 8\n" QK_WISSUE ".endif\n" \
    ".set wl_i, 0\n.rept 8\n" \
    ".set wl_b, 128+(wl_p&3)*32+wl_i*4\n" \
    "v_perm_b32 v120, v[wl_b+2], v[wl_b], s63\nv_perm_b32 v121, v[wl_b+2], v[wl_b], s64\n" \
    "v_perm_b32 v122, v[wl_b+3], v[wl_b+1], s63\nv_perm_b32 v123, v[wl_b+3], v[wl_b+1], s64\n" \
    "v_accvgpr_write_b32 a[wl_p*32+wl_i], v120\nv_accvgpr_write_b32 a[wl_p*32+8+wl_i], v121\n" \
    "v_accvgpr_write_b32 a[wl_p*32+16+wl_i], v122\nv_accvgpr_write_b32 a[wl_p*32+24+wl_i], v123\n" \
    ".set wl_i, wl_i+1\n.endr\n.set wl_p, wl_p+1\n.endr\n"
#define STR2(x) #x
#define STR(x) STR2(x)
#define CLRWAIT(msg) \
    "v_mbcnt_lo_u32_b32 v248, -1, 0\nv_mbcnt_hi_u32_b32 v248, -1, v248\n" \
    "s_mov_b32 s60, " STR(C_EPOCH_) "\ns_mov_b32 s61, " STR(C_MSG_) "\ns_mov_b32 s62, " STR(C_XF_) "\n" \
    "v_lshlrev_b32 v248, 3, v248\nv_and_b32 v249, 0xff, v248\n" \
    "buffer_load_dwordx2 v[250:251], off, %[srd], s60 sc1\n" \
    "Lclr%=:\n" \
    "buffer_load_dwordx2 v[254:255], v248, %[srd], s62 offen sc1\n" \
    ".if " #msg "\nbuffer_load_dwordx2 v[252:253], v249, %[srd], s61 offen sc1\n.endif\n" \
    "s_waitcnt vmcnt(0)\n" \
    "v_readfirstlane_b32 s64, v250\nv_readfirstlane_b32 s65, v251\n" \
    "s_add_u32 s64, s64, 2\ns_addc_u32 s65, s65, 0\ns_or_b32 s64, s64, 1\n" \
    "v_cmp_ne_u32 vcc, s64, v254\n" \
    "v_cmp_ne_u32 s[66:67], s65, v255\n" \
    "s_or_b64 vcc, vcc, s[66:67]\n" \
    ".if " #msg "\nv_or_b32 v247, 1, v252\nv_cmp_ne_u32 s[66:67], s64, v247\ns_or_b64 vcc, vcc, s[66:67]\nv_cmp_ne_u32 s[66:67], s65, v253\ns_or_b64 vcc, vcc, s[66:67]\n.endif\n" \
    "s_cmp_eq_u64 vcc, 0\n" \
    "s_cbranch_scc1 Lclrd%=\n" \
    "s_sub_u32 %[bud], %[bud], 1\n" \
    "s_sleep 4\n" \
    "s_cmp_lt_i32 %[bud], 0\n" \
    "s_cbranch_scc0 Lclr%=\n" \
    "Lclrd%=:\n" \
    ".if " #msg "\n" \
    "v_and_b32 v252, 1, v252\n" \
    "v_cmp_eq_u32 vcc, 1, v252\n" \
    "s_and_b32 s60, vcc_lo, 0x49249249\ns_bcnt1_i32_b32 s60, s60\ns_cmp_gt_u32 s60, 5\ns_cselect_b32 s61, 0x2000, 0\n" \
    "s_and_b32 s60, vcc_lo, 0x92492492\ns_bcnt1_i32_b32 s60, s60\ns_cmp_gt_u32 s60, 5\ns_cselect_b32 s62, 0x2000, 0\n" \
    "s_and_b32 s60, vcc_lo, 0x24924924\ns_bcnt1_i32_b32 s60, s60\ns_cmp_gt_u32 s60, 4\ns_cselect_b32 s63, 0x2000, 0\n" \
    ".endif\n"
#define CLRSO "s_mov_b32 %[so], s61\ns_add_u32 %[so32], s62, 0x200000\n"
#define WAITT(off, L) \
    "s_add_u32 s65, s79, " off "\n" \
    L "a%=:\n" \
    "s_memrealtime s[76:77]\n" \
    "s_waitcnt lgkmcnt(0)\n" \
    "s_sub_i32 s78, s65, s76\n" \
    "s_cmp_gt_i32 s78, 16\n" \
    "s_cbranch_scc1 " L "a%=\n" \
    "s_mul_i32 s60, s78, 3\n" \
    "s_ashr_i32 s60, s60, 2\n" \
    ".p2align 6\n" L "b%=:\ns_sub_u32 s60, s60, 1\ns_cmp_gt_i32 s60, 0\ns_cbranch_scc1 " L "b%=\n"
#define GRID "Lpoll%=:\n" WAITT("0", "Lw") "s_cmp_lt_i32 s78, -10\ns_cselect_b32 s79, s76, s79\n" PT(0)
#define TSLOAD "buffer_load_dword v251, %[tso], %[srd], %[so] offen sc1\n"
#define TSMAX(n) \
    "s_waitcnt vmcnt(" #n ")\n" \
    "v_cmp_eq_u32 vcc, -1, v251\n" \
    "v_subrev_u32 v251, s79, v251\n" \
    "s_nop 1\nv_max_i32_dpp v251, v251, v251 row_ror:8" DPPM \
    "s_nop 1\nv_max_i32_dpp v251, v251, v251 row_ror:4" DPPM \
    "s_nop 1\nv_max_i32_dpp v251, v251, v251 row_ror:2" DPPM \
    "s_nop 1\nv_max_i32_dpp v251, v251, v251 row_ror:1" DPPM \
    "s_nop 0\nv_readlane_b32 s76, v251, 0\nv_readlane_b32 s77, v251, 16\n" \
    "s_max_i32 s76, s76, s77\ns_min_i32 s76, s76, 0x400\n"
#define TSFOLLOW(n) \
    TSMAX(n) \
    "s_add_u32 s76, s76, %[gn]\n" \
    "s_cmp_eq_u64 vcc, 0\n" \
    "s_cselect_b32 s76, s76, %[gd]\n" \
    "s_add_u32 s79, s79, s76\n"
#define MISSUE(b) \
    "buffer_load_dwordx4 v[" #b ":" #b "+3], %[mo], %[srd], %[so] offen sc1\n" \
    "buffer_load_dwordx4 v[" #b "+4:" #b "+7], %[mo], %[srd], %[so] offen offset:1024 sc1\n"
#define MREAD(r) \
    "s_waitcnt lgkmcnt(0)\n" \
    "s_barrier\n" \
    "ds_read_b128 v[" #r ":" #r "+3], %[ap1]\nds_read_b128 v[" #r "+4:" #r "+7], %[ap1] offset:1024\n" \
    "ds_read_b128 v[" #r "+8:" #r "+11], %[ap2]\nds_read_b128 v[" #r "+12:" #r "+15], %[ap2] offset:1024\n" \
    "ds_read_b128 v[" #r "+16:" #r "+19], %[ap3]\nds_read_b128 v[" #r "+20:" #r "+23], %[ap3] offset:1024\n"
#define KRD(i) "ds_read_b128 v[200+" #i "*4:203+" #i "*4], %[kr] offset:" #i "*1024\n"
#define QRD(i) "ds_read_b128 v[176+" #i "*4:179+" #i "*4], %[qr] offset:" #i "*1024\n"
#define KDMA(i) "buffer_load_dwordx4 %[kxo], %[srd], s73 offen offset:" #i "*1024 sc1 lds\n"
#define QK_HEAD(X5, X6, n6) \
    GRID MISSUE(144) \
    "s_waitcnt vmcnt(1)\n" \
    SMF "v[128:131], v[144:147], a[0:7], %[idx]\n" \
    "ds_write_b128 %[mo], v[144:147]\n" \
    "v_max3_u32 v244, v144, v145, v146\n" \
    SMF "v[132:135], v[144:147], a[8:15], %[idx]\n" \
    "s_waitcnt vmcnt(0)\n" \
    "ds_write_b128 %[mo], v[148:151] offset:1024\n" \
    "v_max3_u32 v245, v147, v148, v149\n" \
    "v_max3_u32 v244, v244, v150, v151\n" \
    "v_max_u32 v244, v244, v245\n" \
    "v_cmp_eq_u32 vcc, -1, v244\n" \
    "s_cbranch_vccnz Lretry%=\n" \
    "Lgo%=:\n" PT(1) \
    MREAD(152) \
    SMF "v[136:139], v[144:147], a[16:23], %[idx]\n" SMF "v[140:143], v[144:147], a[24:31], %[idx]\n" \
    Q4("v[148:151]", "32:39", "40:47", "48:55", "56:63", KDMA(0), "") \
    "s_waitcnt lgkmcnt(5)\n" Q4("v[152:155]", "64:71", "72:79", "80:87", "88:95", KRD(0) KRD(1), KDMA(1)) \
    "s_waitcnt lgkmcnt(6)\n" Q4("v[156:159]", "96:103", "104:111", "112:119", "120:127", KRD(2) KRD(3), KDMA(2)) \
    "s_waitcnt lgkmcnt(7)\n" Q4("v[160:163]", "128:135", "136:143", "144:151", "152:159", KRD(4) KRD(5), KDMA(3)) \
    "s_waitcnt lgkmcnt(8)\n" Q4("v[164:167]", "160:167", "168:175", "176:183", "184:191", KRD(6) KRD(7), "") \
    "s_waitcnt lgkmcnt(9)\n" Q4("v[168:171]", "192:199", "200:207", "208:215", "216:223", X5, "") \
    "s_waitcnt lgkmcnt(" #n6 ")\n" Q4("v[172:175]", "224:231", "232:239", "240:247", "248:255", X6, "") \
    "s_nop 1\n" \
    "v_permlane32_swap_b32 v128, v129\nv_permlane32_swap_b32 v130, v131\n" \
    "v_add_f32 v128, v128, v129\nv_add_f32 v130, v130, v131\n" \
    "v_permlane32_swap_b32 v132, v133\nv_permlane32_swap_b32 v134, v135\n" \
    "v_add_f32 v132, v132, v133\nv_add_f32 v134, v134, v135\n" \
    "v_cvt_pk_bf16_f32 v244, v128, v132\nv_cvt_pk_bf16_f32 v246, v130, v134\n" \
    "v_permlane32_swap_b32 v136, v137\nv_permlane32_swap_b32 v138, v139\n" \
    "v_permlane32_swap_b32 v140, v141\nv_permlane32_swap_b32 v142, v143\n" \
    "v_add_f32 v136, v136, v137\nv_add_f32 v140, v140, v141\n" \
    "v_cvt_pk_bf16_f32 v245, v136, v140\n"
#define QK_MID \
    "v_add_f32 v138, v138, v139\nv_add_f32 v142, v142, v143\n" \
    "v_cvt_pk_bf16_f32 v247, v138, v142\n"
#define QK_TAIL(n) \
    PFLUSH TSFOLLOW(n) PT(4) \
    "s_add_u32 %[s], %[s], 1\n" \
    "s_cmp_eq_u32 %[s], 0\n" \
    "s_cselect_b32 s60, 0, 0x10000\n" \
    "s_add_u32 %[so], %[so], s60\n" \
    "s_cmp_eq_u32 %[s], 32\n" \
    "s_cselect_b32 %[so], %[so32], %[so]\n" \
    "s_branch Lpoll%=\n" \
    "Lretry%=:\n" \
    "s_sub_u32 %[bud], %[bud], 1\n" \
    "s_cmp_lt_i32 %[bud], 0\n" \
    "s_cbranch_scc1 Lgo%=\n" \
    "s_add_u32 s79, s79, %[gq]\n" \
    "s_nop 7\n" Z4(128, 129, 130, 131) Z4(132, 133, 134, 135) \
    "s_branch Lpoll%=\n" \
    "Lend%=:\ns_waitcnt vmcnt(0)\n"

#define KQFLOAD \
    "buffer_load_dwordx4 v[244:247], off, %[srd], %[fq] sc1\n" \
    "s_waitcnt vmcnt(0)\n" \
    "v_min3_u32 v244, v244, v245, v246\n" \
    "v_min_u32 v244, v244, v247\n" \
    "s_nop 0\n" \
    "v_readfirstlane_b32 %[seen], v244\n"
#define QK_NEXT \
    "Lnext%=:\n" \
    "s_add_u32 s63, %[s], 1\n" \
    "s_cmp_lg_u32 s73, 0x40000000\n" \
    "s_cbranch_scc1 Lchk%=\n" \
    "Lflag%=:\n" \
    KQFLOAD \
    "s_cmp_gt_u32 %[seen], s63\n" \
    "s_cbranch_scc1 Lgot%=\n" \
    "s_sub_u32 %[bud], %[bud], 1\n" \
    "s_sleep 2\n" \
    "s_cmp_lt_i32 %[bud], 0\n" \
    "s_cbranch_scc0 Lflag%=\n" \
    "Lgot%=:\n" \
    "s_lshl_b32 s73, s63, 18\n" \
    KDMA(0) KDMA(1) KDMA(2) KDMA(3) \
    "s_waitcnt vmcnt(4)\n" \
    "Lchk%=:\n" \
    "s_add_u32 s63, s63, 1\n" \
    "s_cmp_gt_u32 %[seen], s63\n" \
    "s_cbranch_scc1 Lhave%=\n" \
    KQFLOAD \
    "Lhave%=:\n" \
    "s_lshl_b32 s73, s63, 18\n" \
    "s_cmp_gt_u32 %[seen], s63\n" \
    "s_cselect_b32 s73, s73, 0x40000000\n" \
    "s_xor_b32 m0, m0, 0x4000\n" \
    "v_xor_b32 %[kr], 0x4000, %[kr]\n"
#define QK_INIT \
    "s_mov_b32 %[s], -1\ns_mov_b32 s79, %[t0]\ns_mov_b32 s73, 0x40000000\n" \
    "s_lshl_b32 s60, %[w], 12\ns_add_u32 m0, s60, 0x8000\n" \
    "v_mov_b32 v251, -1\n" \
    "s_branch Lnext%=\n"
#define MF "v_mfma_f32_16x16x32_bf16 "

DEV void role_qk(const Params& P, rsrc_t rws, u32x4 srd, char* lds, int h, int w, int lane) {
    const int g = lane >> 4, c = lane & 15;
    const u32x4 rw = mksrd(w < 2 ? P.wq : P.wk, 4u << 20);
    const unsigned wvo = ((g >> 1) + 32 * (g & 1)) * 8192 + h * 256 + (64 * (w & 1) + 4 * c) * 2;
    int budget = SPIN_CAP, s;
    const int idx = (c < 8) ? 0x4444 : 0xEEEE;
    const int rho0 = 4 * (g & 1) + (g >> 1);
    const unsigned mo = w * 2048 + lane * 16;
    const unsigned ap1 = ((2 * w + 2) & 7) * 1024 + lane * 16, ap2 = ((2 * w + 4) & 7) * 1024 + lane * 16, ap3 = ((2 * w + 6) & 7) * 1024 + lane * 16;
    unsigned so, so32;
    unsigned pf = 16384 + w * 2048;
    const unsigned t0 = (unsigned)__builtin_amdgcn_s_memrealtime() - P.dly[0];
    const unsigned tso = lane < 32 ? 3 * 16384 + 4096 + lane * 128 : OOR;
    const unsigned gn = P.dly[0] - P.dly[2] + P.dly[5];
    const unsigned kxo = (w < 2 ? OFF_KF : OFF_QF) + h * 8192 + (w & 1) * 4096 + lane * 16;
    unsigned kr = 32768 + 16384 + lane * 16;
    const unsigned fq = C_KQF + h * 16;
    unsigned seen = 0;
    pf -= 32;
    const unsigned kra0 = 10240 + g * 128 + (((c & 7) + 2 * (g >> 1)) & 7) * 16, kra1 = 10240 + 512 + g * 128 + (((c & 7) + 1 + 2 * (g >> 1)) & 7) * 16;
    if (w < 2) {
        const int ga = 2 * ((c >> 1) & 1) + (c >> 3), i = 2 * ((c >> 2) & 1) + (c & 1);
        const unsigned wa0 = 8192 + w * 1024 + ga * 256 + ((rho0 + 2 * (ga & 1)) & 7) * 16 + i * 4;
        const unsigned wa1 = 8192 + w * 1024 + ga * 256 + ((rho0 + 2 + 2 * (ga & 1)) & 7) * 16 + i * 4;
        const int rq = 4 * w + 2 * ((c >> 2) & 1) + (c & 1);
        const unsigned qra = 8192 + g * 256 + (c >> 3) * 128 + ((rq + 2 * (g & 1)) & 7) * 16;
        const int r = 4 * w + 2 * (g & 1) + (g >> 1);
        unsigned pox = h * 640 + (c >> 3) * 256 + (c & 1) * 128 + r * 16 + ((c >> 1) & 3) * 4;
        unsigned pom = h * 640 + 512 + r * 16 + ((c & 7) >> 1) * 4;
        pox = 16384 * (1 + (pox >> 13)) + (pox & 8191);
        pom = 16384 * (1 + (pom >> 13)) + (pom & 8191);
        asm volatile(
            QK_WLOAD CLRWAIT(1) CLRSO
            QK_INIT
            QK_HEAD("", "", 8)
            "ds_write2_b32 %[wa0], v244, v245 offset1:32\n"
            QK_MID
            "ds_write2_b32 %[wa1], v246, v247 offset1:32\n"
            "s_cmp_eq_u32 %[s], 63\n"
            "s_waitcnt lgkmcnt(0)\n"
            "s_barrier\n" PT(2)
            "s_cbranch_scc1 Lend%=\n"
            "ds_read_b128 v[176:179], %[qra]\nds_read_b128 v[180:183], %[qra] offset:1024\n"
            "ds_read_b128 v[184:187], %[kra0]\nds_read_b128 v[188:191], %[kra1]\n"
            "ds_read_b128 v[192:195], %[kra0] offset:1024\nds_read_b128 v[196:199], %[kra1] offset:1024\n"
            TSLOAD
            "s_waitcnt lgkmcnt(5)\n"
            SMF "v[232:235], v[176:179], v[200:207], %[idx]\n"
            SMF "v[236:239], v[176:179], v[216:223], %[idx]\n"
            "s_waitcnt lgkmcnt(4)\n"
            SMF "v[232:235], v[180:183], v[208:215], %[idx]\n"
            SMF "v[236:239], v[180:183], v[224:231], %[idx]\n"
            "s_waitcnt lgkmcnt(2)\n"
            SMF "v[240:243], v[176:179], v[184:191], %[idx]\n"
            "s_waitcnt lgkmcnt(0)\n"
            SMF "v[240:243], v[180:183], v[192:199], %[idx]\n"
            "v_permlane32_swap_b32 v232, v233\n"
            "v_add_f32 v232, v232, v233\n"
            "v_permlane32_swap_b32 v236, v237\n"
            "v_add_f32 v233, v236, v237\n"
            "v_max_f32 v244, v232, v233\n"
            "s_nop 1\n"
            "v_max_f32_dpp v244, v244, v244 row_ror:8" DPPM
            "v_permlane32_swap_b32 v240, v241\n"
            "v_add_f32 v240, v240, v241\n"
            "v_max_f32_dpp v244, v244, v244 row_ror:4" DPPM
            "s_nop 0\n"
            "v_max_f32_dpp v245, v240, v240 row_ror:4" DPPM
            "v_max_f32_dpp v244, v244, v244 row_ror:2" DPPM
            "s_nop 0\n"
            "v_max_f32_dpp v245, v245, v245 row_ror:2" DPPM
            "v_max_f32_dpp v244, v244, v244 row_ror:1" DPPM
            "s_nop 0\n"
            "v_max_f32_dpp v245, v245, v245 row_ror:1" DPPM
            "v_max_f32 v244, v244, v245\n"
            "v_mul_f32 v244, %[sc], v244\n"
            "v_fma_f32 v232, v232, %[sc], -v244\n"
            "v_fma_f32 v233, v233, %[sc], -v244\n"
            "v_exp_f32 v232, v232\n"
            "v_fma_f32 v240, v240, %[sc], -v244\n"
            "v_exp_f32 v233, v233\n"
            "v_exp_f32 v240, v240\n"
            "v_add_f32 v246, v232, v233\n"
            "v_fmac_f32 v246, 0.5, v240\n"
            "s_nop 0\n"
            "v_mov_b32_dpp v247, v240 quad_perm:[1,0,3,2]" DPPM
            "v_add_f32_dpp v246, v246, v246 row_ror:8" DPPM
            "v_cndmask_b32 v248, v240, v247, %[odd]\n"
            "v_cndmask_b32 v249, v247, v240, %[odd]\n"
            "v_add_f32_dpp v246, v246, v246 row_ror:4" DPPM
            "s_nop 1\n"
            "v_add_f32_dpp v246, v246, v246 row_ror:2" DPPM
            "s_nop 1\n"
            "v_add_f32_dpp v246, v246, v246 row_ror:1" DPPM
            "v_rcp_f32 v246, v246\n"
            "s_nop 0\n"
            "v_pk_mul_f32 v[232:233], v[232:233], v[246:247] op_sel_hi:[1,0] clamp\n"
            "v_cvt_pk_bf16_f32 v232, v232, v233\n"
            "buffer_store_dword v232, %[pox], %[srd], %[so] offen sc1\n"
            "v_pk_mul_f32 v[248:249], v[248:249], v[246:247] op_sel_hi:[1,0] clamp\n"
            "v_cvt_pk_bf16_f32 v248, v248, v249\n"
            "buffer_store_dword v248, %[pom], %[srd], %[so] offen sc1\n" PT(3)
            QK_NEXT
            QZERO SZERO
            QK_TAIL(2)
            : [s] "=&s"(s), [so] "=&s"(so), [so32] "=&s"(so32), [bud] "+s"(budget), [seen] "+s"(seen), [pf] "+v"(pf), [kr] "+v"(kr)
            : [rw] "s"(rw), [wvo] "v"(wvo), [srd] "s"(srd), [mo] "v"(mo), [idx] "v"(idx), [ap1] "v"(ap1), [ap2] "v"(ap2), [ap3] "v"(ap3), [wa0] "v"(wa0), [wa1] "v"(wa1),
              [qra] "v"(qra), [kra0] "v"(kra0), [kra1] "v"(kra1), [pox] "v"(pox), [pom] "v"(pom), [kxo] "v"(kxo),
              [gd] "s"(P.dly[0]), [gq] "s"(P.dly[8]), [gn] "s"(gn), [tso] "v"(tso), [t0] "s"(t0), [fq] "s"(fq), [sc] "s"(SC_LOG2), [odd] "s"(0xAAAAAAAAAAAAAAAAull), [w] "s"(w)
            : ACL, CL8(12), CL8(13), CL8(14), CL8(15), CL8(16), CL8(17), CL8(18), CL8(19), CL8(20), CL8(21), CL8(22), CL8(23), CL8(24),
              "v250", "v251", "v252", "v253", "v254", "v255", SCL, "vcc", "scc", "memory");
    } else {
        const int rot = ((c >> 1) & 1) + 2 * (c >> 3);
        const unsigned wa0 = 10240 + (w - 2) * 1024 + ((c >> 1) & 1) * 512 + (c >> 2) * 128 + ((rho0 + rot) & 7) * 16 + (c & 1) * 8;
        const unsigned wa1 = 10240 + (w - 2) * 1024 + ((c >> 1) & 1) * 512 + (c >> 2) * 128 + ((rho0 + 2 + rot) & 7) * 16 + (c & 1) * 8;
        const int tt = w - 2, tq = 16 * tt + c - 4 * g;
        u32x8 cm;
#pragma unroll
        for (int r = 0; r < 4; ++r) { cm[r] = r <= tq ? 0u : asu(-1.0e30f); cm[4 + r] = 16 + r <= tq ? 0u : asu(-1.0e30f); }
        unsigned qr = 32768 + 16384 + 8192 + tt * 4096 + lane * 16;
        const unsigned pxo = OFF_P + tt * 40960 + h * 1024 + lane * 16;
        const unsigned pmo = OFF_P + tt * 40960 + (32 + (h >> 2)) * 1024 + ((h & 3) * 16 + c) * 16 + (g & 1) * 8;
        const unsigned fk = (w == 2 && lane == 0) ? C_PF + h * 4 : OOR;
        asm volatile(
            QK_WLOAD CLRWAIT(1) CLRSO
            QK_INIT
            QK_HEAD(QRD(0) QRD(1), QRD(2) QRD(3), 10)
            "ds_write_b64 %[wa0], v[244:245]\n"
            QK_MID
            "ds_write_b64 %[wa1], v[246:247]\n"
            "s_waitcnt lgkmcnt(0)\n"
            "s_barrier\n" PT(2)
            MF "v[232:235], v[200:203], v[176:179], v[112:115]\n"
            TSLOAD
            MF "v[236:239], v[216:219], v[176:179], v[116:119]\n"
            MF "v[232:235], v[204:207], v[180:183], v[232:235]\n"
            MF "v[236:239], v[220:223], v[180:183], v[236:239]\n"
            MF "v[232:235], v[208:211], v[184:187], v[232:235]\n"
            MF "v[236:239], v[224:227], v[184:187], v[236:239]\n"
            "ds_read_b128 v[96:99], %[kra0]\nds_read_b128 v[100:103], %[kra1]\n"
            "ds_read_b128 v[104:107], %[kra0] offset:1024\nds_read_b128 v[108:111], %[kra1] offset:1024\n"
            MF "v[232:235], v[212:215], v[188:191], v[232:235]\n"
            MF "v[236:239], v[228:231], v[188:191], v[236:239]\n"
            "s_mul_i32 s61, %[s], 81920\n"
            "s_waitcnt lgkmcnt(0)\n"
            MF "v[240:243], v[96:99], v[176:179], 0\n"
            MF "v[240:243], v[100:103], v[180:183], v[240:243]\n"
            MF "v[240:243], v[104:107], v[184:187], v[240:243]\n"
            MF "v[240:243], v[108:111], v[188:191], v[240:243]\n"
            "v_max3_f32 v244, v232, v233, v234\n"
            "v_max3_f32 v245, v235, v236, v237\n"
            "v_max3_f32 v244, v244, v238, v239\n"
            "v_max_f32 v244, v244, v245\n"
            "s_nop 4\n"
            "v_max3_f32 v245, v240, v241, v242\n"
            "v_max3_f32 v244, v244, v245, v243\n"
            "v_mov_b32 v245, v244\n"
            "s_nop 1\n"
            "v_permlane16_swap_b32 v244, v245\n"
            "v_max_f32 v244, v244, v245\n"
            "v_mov_b32 v245, v244\n"
            "s_nop 1\n"
            "v_permlane32_swap_b32 v244, v245\n"
            "v_max_f32 v244, v244, v245\n"
            "v_mul_f32 v244, %[sc], v244\n"
            ".set wl_i, 232\n.rept 12\nv_fma_f32 v[wl_i], v[wl_i], %[sc], -v244\n.set wl_i, wl_i+1\n.endr\n"
            ".set wl_i, 232\n.rept 12\nv_exp_f32 v[wl_i], v[wl_i]\n.set wl_i, wl_i+1\n.endr\n"
            "v_pk_add_f32 v[246:247], v[232:233], v[234:235]\n"
            "v_pk_add_f32 v[248:249], v[236:237], v[238:239]\n"
            "v_pk_add_f32 v[244:245], v[240:241], v[242:243]\n"
            "v_pk_add_f32 v[246:247], v[246:247], v[248:249]\n"
            "v_add_f32 v244, v244, v245\n"
            "v_add_f32 v246, v246, v247\n"
            "v_fmac_f32 v246, 0.5, v244\n"
            "v_mov_b32 v247, v246\n"
            "s_nop 1\n"
            "v_permlane16_swap_b32 v246, v247\n"
            "v_add_f32 v246, v246, v247\n"
            "v_mov_b32 v247, v246\n"
            "s_nop 1\n"
            "v_permlane32_swap_b32 v246, v247\n"
            "v_add_f32 v246, v246, v247\n"
            "v_rcp_f32 v246, v246\n"
            "v_mov_b32 v244, %[s]\n"
            ".set wl_i, 232\n.rept 6\nv_pk_mul_f32 v[wl_i:wl_i+1], v[wl_i:wl_i+1], v[246:247] op_sel_hi:[1,0]\n.set wl_i, wl_i+2\n.endr\n"
            "v_cvt_pk_bf16_f32 v192, v232, v236\nv_cvt_pk_bf16_f32 v193, v233, v237\n"
            "v_cvt_pk_bf16_f32 v194, v234, v238\nv_cvt_pk_bf16_f32 v195, v235, v239\n"
            "v_cvt_pk_bf16_f32 v196, v240, v241\nv_cvt_pk_bf16_f32 v197, v242, v243\n"
            "buffer_store_dwordx4 v[192:195], %[pxo], %[srd], s61 offen sc1\n"
            "buffer_store_dwordx2 v[196:197], %[pmo], %[srd], s61 offen sc1\n"
            "s_cmp_eq_u32 %[s], 63\n"
            "s_cbranch_scc1 Lend%=\n"
            "buffer_store_dword v244, %[fk], %[srd], 0 offen sc1\n" PT(3)
            QK_NEXT
            "v_xor_b32 %[qr], 0x4000, %[qr]\n"
            QZERO
            QK_TAIL(3)
            : [s] "=&s"(s), [so] "=&s"(so), [so32] "=&s"(so32), [bud] "+s"(budget), [seen] "+s"(seen), [pf] "+v"(pf), [kr] "+v"(kr), [qr] "+v"(qr)
            : [rw] "s"(rw), [wvo] "v"(wvo), [srd] "s"(srd), [mo] "v"(mo), [idx] "v"(idx), [ap1] "v"(ap1), [ap2] "v"(ap2), [ap3] "v"(ap3), [wa0] "v"(wa0), [wa1] "v"(wa1),
              [kra0] "v"(kra0), [kra1] "v"(kra1), [kxo] "v"(kxo), [pxo] "v"(pxo), [pmo] "v"(pmo), "{v[112:119]}"(cm),
              [gd] "s"(P.dly[0]), [gq] "s"(P.dly[8]), [gn] "s"(gn), [tso] "v"(tso), [t0] "s"(t0), [fq] "s"(fq), [fk] "v"(fk), [sc] "s"(SC_LOG2), [w] "s"(w)
            : ACL, "v96", "v97", "v98", "v99", CL8(10), "v110", "v111", CL8(12), CL8(13), CL8(14), CL8(15), CL8(16), CL8(17), CL8(18), CL8(19), CL8(20), CL8(21),
              CL8(22), CL8(23), CL8(24), "v250", "v251", "v252", "v253", "v254", "v255", SCL, "vcc", "scc", "memory");
    }
    WAIT_VM0();
    BAR();
    if (threadIdx.x == 0) st32<SC1>(NSEG, rws, C_PF + h * 4, 0);
    if (budget < 0 && lane == 0) st32<SC1>(1, rws, C_ERR, 0);
    STAMP(128 + h);
    PDUMP(h);
}

#define V2(A, b0, b1) SMF "v[128:131], " A ", a[" b0 "], %[idx]\n" SMF "v[132:135], " A ", a[" b1 "], %[idx]\n"
#define VWL(r, o, so) "buffer_load_dwordx4 v[" r "], %[vwo], %[srd], " so " offen offset:" o " sc1\n"
#define UM(d, A, b) SMF "v[" d "], " A ", a[" b "], %[idx]\n"
#define F0 "v[168:171]"
#define F1 "v[172:175]"
#define SW(a, b) "v_permlane32_swap_b32 v" #a ", v" #b "\n"
#define S16(a, b) "v_permlane16_swap_b32 v" #a ", v" #b "\n"
#define AD(a, b) "v_add_f32 v" #a ", v" #a ", v" #b "\n"
#define CV(d, a, b) "v_cvt_pk_bf16_f32 v" #d ", v" #a ", v" #b "\n"
#define MN(d) "v_min_u32 v" #d ", -2, v" #d "\n"
#define VZERO Z4(128, 129, 130, 131) Z4(132, 133, 134, 135)
#define UZERO Z4(176, 177, 178, 179) Z4(180, 181, 182, 183) Z4(184, 185, 186, 187) Z4(188, 189, 190, 191) \
              Z4(192, 193, 194, 195) Z4(196, 197, 198, 199) Z4(200, 201, 202, 203) Z4(204, 205, 206, 207)
#define VO_FLAG(L) \
    "Lflag" L "%=:\n" \
    "buffer_load_dword v248, %[fvo], %[srd], 0 offen sc1\n" \
    "s_waitcnt vmcnt(0)\n" \
    "v_min_u32_dpp v248, v248, v248 row_ror:4" DPPM "s_nop 1\n" \
    "v_min_u32_dpp v248, v248, v248 row_ror:2" DPPM "s_nop 1\n" \
    "v_min_u32_dpp v248, v248, v248 row_ror:1" DPPM "s_nop 0\n" \
    "v_readfirstlane_b32 %[seen], v248\n" \
    "s_cmp_gt_u32 %[seen], s69\n" \
    "s_cbranch_scc1 Lhave" L "%=\n" \
    "s_sub_u32 %[bud], %[bud], 1\n" \
    "s_cmp_lt_i32 %[bud], 0\n" \
    "s_cbranch_scc1 Lhave" L "%=\n" \
    "s_sleep 2\n" \
    "s_branch Lflag" L "%=\n" \
    "Lhave" L "%=:\n"
#define VO_VISSUE \
    "s_add_u32 s61, s59, wl_t/2\ns_and_b32 s61, s61, 3\ns_lshl_b32 s61, s61, 20\n" \
    ".if wl_t < 2\n.set wl_d, 96+wl_t*16\n.else\n.set wl_d, 96+wl_t*16\n.endif\n" \
    ".set wl_i, 0\n.rept 16\n" \
    "s_add_u32 s62, s61, ((wl_t&1)*4 + 2*(wl_i>>3) + 64*((wl_i>>1)&1) + 16*((wl_i>>2)&1) + 8*(wl_i&1))*8192\n" \
    "buffer_load_dword v[wl_d+wl_i], %[wvo], %[rv], s62 offen\n" \
    ".set wl_i, wl_i+1\n.endr\n.set wl_t, wl_t+1\n"
#define VO_WLOAD \
    "s_mov_b32 s63, 0x05040100\ns_mov_b32 s64, 0x07060302\n" \
    ".set wl_i, 0\n.rept 32\n" \
    "s_mov_b32 s62, (64*(wl_i>>4) + 32*((wl_i>>3)&1) + 8*((wl_i>>1)&1) + 2*((wl_i>>2)&1))*1024\n" \
    "buffer_load_dwordx4 v[128+wl_i*4:131+wl_i*4], %[woo], %[ro], s62 offen offset:(wl_i&1)*1024\n" \
    ".set wl_i, wl_i+1\n.endr\n" \
    ".set wl_t, 0\n" VO_VISSUE \
    ".set wl_p, 0\n.rept 2\n" \
    "s_waitcnt vmcnt(32)\n" VO_VISSUE \
    ".set wl_i, 0\n.rept 8\n.set wl_k, 0\n.rept 4\n" \
    ".set wl_b, 128+wl_p*64+wl_i*8+wl_k\n" \
    "v_perm_b32 v84, v[wl_b+4], v[wl_b], s63\nv_perm_b32 v85, v[wl_b+4], v[wl_b], s64\n" \
    "v_accvgpr_write_b32 a[128+wl_k*32+wl_p*8+wl_i], v84\nv_accvgpr_write_b32 a[128+wl_k*32+16+wl_p*8+wl_i], v85\n" \
    ".set wl_k, wl_k+1\n.endr\n.set wl_i, wl_i+1\n.endr\n" \
    ".set wl_p, wl_p+1\n.endr\n" \
    ".set wl_p, 0\n.rept 8\n" \
    ".if wl_p < 6\ns_waitcnt vmcnt(32)\n.elseif wl_p == 6\ns_waitcnt vmcnt(16)\n.else\ns_waitcnt vmcnt(0)\n.endif\n" \
    ".if wl_t < 8\n" VO_VISSUE ".endif\n" \
    ".set wl_i, 0\n.rept 8\n" \
    ".set wl_b, 96+wl_p*16+wl_i*2\n" \
    "v_perm_b32 v84, v[wl_b+1], v[wl_b], s63\nv_perm_b32 v85, v[wl_b+1], v[wl_b], s64\n" \
    "v_accvgpr_write_b32 a[wl_p*16+wl_i], v84\nv_accvgpr_write_b32 a[wl_p*16+8+wl_i], v85\n" \
    ".set wl_i, wl_i+1\n.endr\n.set wl_p, wl_p+1\n.endr\n"
DEV void role_vo(const Params& P, rsrc_t rws, u32x4 srd, char* lds, int h, int w, int lane) {
    const int g = lane >> 4, c = lane & 15;
    const u32x4 rv = mksrd(P.wv, 4u << 20), ro = mksrd(P.wo, 4u << 20);
    const unsigned wvo = ((g >> 1) + 32 * (g & 1)) * 8192 + h * 256 + (32 * w + 2 * c) * 2;
    const unsigned woo = (h * 128 + 16 * (g >> 1) + 4 * (g & 1)) * 1024 + (128 * w + 8 * c) * 2;
    int budget = SPIN_CAP, s;
    const int idx = (c < 8) ? 0x4444 : 0xEEEE;
    const int rho0 = 4 * (g & 1) + (g >> 1);
    const int vga = 2 * (w & 1) + (c >> 3);
    const unsigned va0 = 8192 + (w >> 1) * 1024 + (vga * 16 + ((c >> 2) & 1) * 8 + ((rho0 + 2 * (vga & 1)) & 7)) * 16 + (c & 3) * 4;
    const unsigned va1 = 8192 + (w >> 1) * 1024 + (vga * 16 + ((c >> 2) & 1) * 8 + ((rho0 + 2 + 2 * (vga & 1)) & 7)) * 16 + (c & 3) * 4;
    const unsigned vra = 8192 + (g * 16 + (c & 8) + (((c & 7) + 2 * (g & 1)) & 7)) * 16;
    const unsigned mo = w * 2048 + lane * 16;
    const unsigned ap1 = ((2 * w + 2) & 7) * 1024 + lane * 16, ap2 = ((2 * w + 4) & 7) * 1024 + lane * 16, ap3 = ((2 * w + 6) & 7) * 1024 + lane * 16;
    unsigned uso = OFF_URING + (8 * w + 2 * (g & 1) + (lane >> 5)) * 16384 + h * 256 + c * 16;
    unsigned so, so32;
    unsigned pf = 16384 + w * 2048;
    const unsigned t0 = (unsigned)__builtin_amdgcn_s_memrealtime();
    const unsigned tso = lane < 32 ? 3 * 16384 + 4096 + lane * 128 : OOR;
    const unsigned gn = P.dly[0] - P.dly[2] + P.dly[4];
    asm volatile(
        "s_mov_b32 s59, %[w]\n"
        VO_WLOAD CLRWAIT(1) CLRSO
        "v_add_u32 %[uso], s63, %[uso]\n"
        "s_mov_b32 %[s], 0\ns_mov_b32 s79, %[t0]\n" VZERO UZERO
        "s_mov_b32 s63, 0\n"
        GRID MISSUE(136)
        "s_waitcnt vmcnt(1)\n"
        SMF "v[128:131], v[136:139], a[0:7], %[idx]\n"
        "ds_write_b128 %[mo], v[136:139]\n"
        "v_max3_u32 v248, v136, v137, v138\n"
        SMF "v[132:135], v[136:139], a[8:15], %[idx]\n"
        "s_waitcnt vmcnt(0)\n"
        "ds_write_b128 %[mo], v[140:143] offset:1024\n"
        "v_max3_u32 v249, v139, v140, v141\n"
        "v_max3_u32 v248, v248, v142, v143\n"
        "v_max_u32 v248, v248, v249\n"
        "v_cmp_eq_u32 vcc, -1, v248\n"
        "s_cbranch_vccnz Lretry%=\n"
        "Lgo%=:\n" PT(1)
        MREAD(144)
        V2("v[140:143]", "16:23", "24:31")
        "s_waitcnt lgkmcnt(5)\n" V2("v[144:147]", "32:39", "40:47")
        "s_waitcnt lgkmcnt(4)\n" V2("v[148:151]", "48:55", "56:63")
        "s_waitcnt lgkmcnt(3)\n" V2("v[152:155]", "64:71", "72:79")
        "s_waitcnt lgkmcnt(2)\n" V2("v[156:159]", "80:87", "88:95")
        "s_waitcnt lgkmcnt(1)\n" V2("v[160:163]", "96:103", "104:111")
        "s_waitcnt lgkmcnt(0)\n" V2("v[164:167]", "112:119", "120:127")
        "s_add_u32 s67, s63, 0x10000\ns_nop 2\n"
        SW(128, 129) SW(130, 131) AD(128, 129) AD(130, 131)
        SW(132, 133) SW(134, 135) AD(132, 133) CV(248, 128, 132)
        "ds_write_b32 %[va0], v248\n"
        AD(134, 135) CV(249, 130, 134)
        "ds_write_b32 %[va1], v249\n"
        "s_waitcnt lgkmcnt(0)\n"
        "s_barrier\n"
        "ds_read_b128 v[168:171], %[vra]\nds_read_b128 v[172:175], %[vra] offset:1024\n"
        TSLOAD
        VZERO
        "s_waitcnt lgkmcnt(1)\n"
        UM("176:179", F0, "128:135") UM("180:183", F0, "144:151")
        "s_waitcnt lgkmcnt(0)\n"
        UM("176:179", F1, "136:143") UM("180:183", F1, "152:159")
        UM("184:187", F0, "160:167") UM("188:191", F0, "176:183")
        SW(176, 180) SW(177, 181) SW(178, 182)
        UM("184:187", F1, "168:175")
        SW(179, 183) AD(176, 180) AD(177, 181)
        UM("188:191", F1, "184:191")
        AD(178, 182) AD(179, 183) CV(240, 176, 177)
        UM("192:195", F0, "192:199")
        CV(241, 178, 179) MN(240) MN(241)
        UM("196:199", F0, "208:215")
        SW(184, 188) SW(185, 189) SW(186, 190)
        UM("192:195", F1, "200:207")
        SW(187, 191) AD(184, 188) AD(185, 189)
        UM("196:199", F1, "216:223")
        AD(186, 190) AD(187, 191) CV(242, 184, 185)
        UM("200:203", F0, "224:231")
        CV(243, 186, 187) MN(242) MN(243)
        UM("204:207", F0, "240:247")
        SW(192, 196) S16(240, 242) S16(241, 243)
        UM("200:203", F1, "232:239")
        "buffer_store_dwordx4 v[240:243], %[uso], %[srd], s63 offen sc1\n"
        SW(193, 197) SW(194, 198)
        UM("204:207", F1, "248:255")
        SW(195, 199) AD(192, 196) AD(193, 197) AD(194, 198) AD(195, 199) CV(244, 192, 193) CV(245, 194, 195) MN(244)
        SW(200, 204) SW(201, 205) SW(202, 206) SW(203, 207) AD(200, 204) AD(201, 205) AD(202, 206) AD(203, 207)
        CV(246, 200, 201) CV(247, 202, 203) MN(246) MN(247) MN(245)
        S16(244, 246) "s_nop 0\n" S16(245, 247)
        "buffer_store_dwordx4 v[244:247], %[uso], %[srd], s67 offen sc1\n" PT(2)
        "s_cmp_eq_u32 %[s], 63\n"
        "s_cbranch_scc1 Lend%=\n"
        UZERO
        TSFOLLOW(2)
        PFLUSH
        "s_add_u32 %[s], %[s], 1\n"
        "s_add_u32 %[so], %[so], 0x10000\n"
        "s_cmp_eq_u32 %[s], 32\n"
        "s_cselect_b32 %[so], %[so32], %[so]\n"
        "s_add_u32 s63, s63, 0x80000\ns_and_b32 s63, s63, 0x180000\n"
        "s_branch Lpoll%=\n"
        "Lretry%=:\n"
        "s_sub_u32 %[bud], %[bud], 1\n"
        "s_cmp_lt_i32 %[bud], 0\n"
        "s_cbranch_scc1 Lgo%=\n"
        "s_add_u32 s79, s79, %[gq]\n"
        "s_nop 7\n" VZERO
        "s_branch Lpoll%=\n"
        "Lend%=:\ns_waitcnt vmcnt(0)\n"
        : [s] "=&s"(s), [so] "=&s"(so), [so32] "=&s"(so32), [bud] "+s"(budget), [pf] "+v"(pf), [uso] "+v"(uso)
        : [rv] "s"(rv), [ro] "s"(ro), [wvo] "v"(wvo), [woo] "v"(woo),
          [srd] "s"(srd), [mo] "v"(mo), [idx] "v"(idx), [ap1] "v"(ap1), [ap2] "v"(ap2), [ap3] "v"(ap3), [va0] "v"(va0), [va1] "v"(va1),
          [vra] "v"(vra),
          [gd] "s"(P.dly[0]), [gq] "s"(P.dly[8]), [gn] "s"(gn), [tso] "v"(tso), [t0] "s"(t0), [w] "s"(w)
        : ACL, "v84", "v85", "v96", "v97", "v98", "v99", CL8(10), CL8(11), CL8(12), CL8(13), CL8(14), CL8(15), CL8(16), CL8(17), CL8(18), CL8(19), CL8(20), CL8(21),
          CL8(22), CL8(23), CL8(24), "v250", "v251", "v252", "v253", "v254", "v255", "s59", SCL, "vcc", "scc", "memory");
    if (budget < 0 && lane == 0) st32<SC1>(2, rws, C_ERR, 0);
    STAMP(160 + h);
    PDUMP(32 + h);
}

#define PLOAD \
    "buffer_load_dwordx4 v[216:219], v88, %[srd], %[so] offen sc1\n" \
    "buffer_load_dwordx4 v[220:223], v89, %[srd], %[so] offen sc1\n" \
    "buffer_load_dwordx4 v[224:227], v90, %[srd], %[so] offen sc1\n" \
    "buffer_load_dwordx4 v[228:231], v91, %[srd], %[so] offen sc1\n" \
    "buffer_load_dwordx4 v[232:235], v92, %[srd], %[so] offen sc1\n"
#define ULOAD \
    "buffer_load_dwordx4 v[208:211], %[ulo], %[srd], s63 offen sc1\n" \
    "buffer_load_dwordx4 v[212:215], %[ulo], %[srd], s63 offen offset:1024 sc1\n"
#define VWALL VWL("96:99", "0", "s61") VWL("100:103", "1024", "s61") VWL("104:107", "2048", "s61") VWL("108:111", "3072", "s61") \
              VWL("112:115", "0", "s62") VWL("116:119", "1024", "s62") VWL("120:123", "2048", "s62") VWL("124:127", "3072", "s62")
DEV void role_ow(const Params& P, rsrc_t rws, u32x4 srd, char* lds, int h, int w, int lane) {
    const int g = lane >> 4, c = lane & 15;
    int budget = SPIN_CAP, s;
    const int idx = (c < 8) ? 0x4444 : 0xEEEE;
    unsigned ulo = OFF_URING + h * 16384 + w * 2048 + lane * 16;
    u32x8 pl;
    {
        const int hf = (c >> 3), r = c & 7;
#pragma unroll
        for (int n = 0; n < 5; ++n) {
            unsigned o = n < 4 ? (8 * w + 2 * n + (g >> 1)) * 640 + (g & 1) * 256 + hf * 128 + r * 16
                               : (8 * w + 2 * g + hf) * 640 + 512 + r * 16;
            pl[n] = 16384 * (1 + (o >> 13)) + (o & 8191);
        }
        pl[5] = w * 512 + ((g >> 1) * 16 + (g & 1) * 32 + c) * 4;
        const int rr = (lane >> 2) & 7;
        pl[6] = ((rr & 1) * 16 + (rr >> 2) * 32 + ((rr >> 1) & 1) * 64 + 8 * (lane >> 5) + 2 * (lane & 3)) * 4;
        pl[7] = h * 256 + lane * 4;
    }
    const unsigned vwo = OFF_VW + (h * 32 + 8 * w) * 1024 + lane * 16;
    const unsigned fvo = C_VWF + (h >> 3) * 128 + w * 32 + (lane & 7) * 4;
    unsigned so, so32;
    unsigned pf = 16384 + w * 2048;
    const unsigned t0 = (unsigned)__builtin_amdgcn_s_memrealtime();
    const unsigned tso = lane < 32 ? 3 * 16384 + 4096 + lane * 128 : OOR, tsw = lane == 0 ? 3 * 16384 + 4096 + h * 128 : OOR;
    unsigned seen = 0;
    asm volatile(
        CLRWAIT(1) CLRSO
        "v_add_u32 %[ulo], s63, %[ulo]\n"
        "s_mov_b32 s69, 0\n"
        VO_FLAG("A")
        "s_mov_b32 %[s], 0\ns_mov_b32 s79, %[t0]\n" Z4(236, 237, 238, 239)
        "s_mov_b32 s61, 0\ns_movk_i32 s62, 0x1000\ns_mov_b32 s63, 0\n"
        VWALL
        "s_memrealtime s[76:77]\ns_waitcnt lgkmcnt(0)\ns_mov_b32 s79, s76\ns_mov_b32 s74, s76\n"
        GRID
        "buffer_load_dwordx4 v[216:219], v88, %[srd], %[so] offen sc1\n"
        "buffer_load_dwordx4 v[220:223], v89, %[srd], %[so] offen sc1\n"
        "buffer_load_dwordx4 v[224:227], v90, %[srd], %[so] offen sc1\n"
        "buffer_load_dwordx4 v[232:235], v92, %[srd], %[so] offen sc1\n"
        ULOAD
        "buffer_load_dwordx4 v[228:231], v91, %[srd], %[so] offen sc1\n"
        "s_waitcnt vmcnt(6)\n"
        SMF "v[236:239], v[216:219], v[96:103], %[idx]\n"
        "v_max3_u32 v248, v216, v217, v218\n"
        "s_waitcnt vmcnt(5)\n"
        SMF "v[236:239], v[220:223], v[104:111], %[idx]\n"
        "v_max3_u32 v249, v219, v220, v221\nv_max3_u32 v248, v248, v222, v223\n"
        "s_waitcnt vmcnt(4)\n"
        SMF "v[236:239], v[224:227], v[112:119], %[idx]\n"
        "v_max3_u32 v249, v249, v224, v225\nv_max3_u32 v248, v248, v226, v227\n"
        "s_waitcnt vmcnt(3)\n"
        "v_max3_u32 v249, v249, v232, v233\nv_max3_u32 v248, v248, v234, v235\n"
        "s_waitcnt vmcnt(2)\n"
        "v_max3_u32 v249, v249, v208, v209\nv_max3_u32 v248, v248, v210, v211\n"
        S16(208, 210) S16(209, 211) "v_swap_b32 v209, v210\n"
        "s_waitcnt vmcnt(1)\n"
        "v_max3_u32 v249, v249, v212, v213\nv_max3_u32 v248, v248, v214, v215\n"
        S16(212, 214) S16(213, 215) "v_swap_b32 v213, v214\n"
        "s_waitcnt vmcnt(0)\n"
        SMF "v[236:239], v[228:231], v[120:127], %[idx]\n"
        "v_max3_u32 v249, v249, v228, v229\n"
        SMF "v[236:239], v[232:235], v[208:215], %[idx]\n"
        "v_max3_u32 v248, v248, v230, v231\n"
        "s_nop 0\n"
        "v_max_u32 v248, v248, v249\n"
        "v_cmp_eq_u32 vcc, -1, v248\n"
        "s_cbranch_vccnz Lretry%=\n"
        "Lgo%=:\n" PT(4)
        "s_add_u32 s70, %[so], 0x10000\n"
        "s_cmp_eq_u32 %[s], 31\n"
        "s_cselect_b32 s70, %[so32], s70\n"
        "s_cmp_lg_u32 %[w], 0\n"
        SW(236, 237) SW(238, 239) AD(236, 237) AD(238, 239)
        "ds_write2_b32 v93, v236, v238 offset1:64\n"
        "s_waitcnt lgkmcnt(0)\n"
        "s_barrier\n"
        "s_cbranch_scc1 Lnom%=\n"
        "ds_read2_b64 v[216:219], v94 offset1:64\n"
        "ds_read2_b64 v[220:223], v94 offset0:128 offset1:192\n"
        "s_waitcnt lgkmcnt(1)\n"
        "v_pk_add_f32 v[216:217], v[216:217], v[218:219]\n"
        "s_waitcnt lgkmcnt(0)\n"
        "v_pk_add_f32 v[220:221], v[220:221], v[222:223]\n"
        "v_pk_add_f32 v[216:217], v[216:217], v[220:221]\n"
        CV(216, 216, 217) MN(216)
        "buffer_store_dword v216, v95, %[srd], s70 offen sc1\n"
        "Lnom%=:\n" PT(5)
        "s_cmp_eq_u32 %[s], 62\n"
        "s_cbranch_scc1 Lend%=\n"
        "s_cmp_lg_u32 %[w], 1\n"
        "s_cbranch_scc1 Lnot%=\n"
        "s_memrealtime s[76:77]\n"
        "s_add_u32 s60, s74, %[gd]\n"
        "s_add_u32 s65, s74, %[th]\n"
        "s_waitcnt lgkmcnt(0)\n"
        "s_sub_i32 s78, s76, s65\n"
        "s_add_u32 s76, s76, %[gx]\n"
        "s_cmp_gt_i32 s78, 0\n"
        "s_cselect_b32 s60, s76, s60\n"
        "s_min_u32 s60, s60, -2\n"
        "v_mov_b32 v216, s60\n"
        "buffer_store_dword v216, %[tsw], %[srd], s70 offen sc1\n"
        "Lnot%=:\n"
        Z4(236, 237, 238, 239)
        PFLUSH
        "s_add_u32 %[s], %[s], 1\n"
        "s_mov_b32 %[so], s70\n"
        "s_mov_b32 s69, %[s]\n"
        "s_lshl_b32 s61, s69, 20\n"
        "s_add_u32 s62, s61, 0x1000\n"
        "s_mov_b32 s79, s74\n"
        WAITT("%[dr]", "Lv") PT(2)
        TSLOAD
        "buffer_load_dword v252, off, %[srd], %[fra] sc1\n"
        "s_cmp_gt_u32 %[seen], s69\n"
        "s_cbranch_scc1 LhaveB%=\n"
        VO_FLAG("B")
        VWALL
        "s_add_u32 s63, s63, 0x80000\ns_and_b32 s63, s63, 0x180000\n"
        "s_add_u32 s79, s79, %[gd]\n"
        "s_waitcnt vmcnt(8)\n"
        "Lra%=:\n"
        "v_readfirstlane_b32 s60, v252\n"
        "s_add_u32 s60, s60, 3\n"
        "s_cmp_gt_u32 s60, %[s]\n"
        "s_cbranch_scc1 Lrad%=\n"
        "s_sleep 2\n"
        "buffer_load_dword v252, off, %[srd], %[fra] sc1\n"
        "s_sub_u32 %[bud], %[bud], 1\n"
        "s_waitcnt vmcnt(0)\n"
        "s_cmp_lt_i32 %[bud], 0\n"
        "s_cbranch_scc0 Lra%=\n"
        "Lrad%=:\n"
        TSMAX(8) PT(3)
        "s_cbranch_vccz Ltsok%=\n"
        WAITT("-45", "Lt")
        "Lts%=:\n"
        TSLOAD TSMAX(0)
        "s_cbranch_vccz Ltsok%=\n"
        "s_sub_u32 %[bud], %[bud], 1\n"
        "s_cmp_lt_i32 %[bud], 0\n"
        "s_cbranch_scc0 Lts%=\n"
        "Ltsok%=:\n"
        "s_max_i32 s76, s76, 0\n"
        "s_add_u32 s79, s79, s76\n"
        "s_mov_b32 s74, s79\n"
        "s_branch Lpoll%=\n"
        "Lretry%=:\n"
        "s_sub_u32 %[bud], %[bud], 1\n"
        "s_cmp_lt_i32 %[bud], 0\n"
        "s_cbranch_scc1 Lgo%=\n"
        "s_add_u32 s79, s79, %[gq]\n"
        "s_nop 7\n" Z4(236, 237, 238, 239)
        "s_branch Lpoll%=\n"
        "Lend%=:\ns_waitcnt vmcnt(0)\n"
        : [s] "=&s"(s), [so] "=&s"(so), [so32] "=&s"(so32), [bud] "+s"(budget), [seen] "+s"(seen), [pf] "+v"(pf), [ulo] "+v"(ulo)
        : "{v[88:95]}"(pl), [srd] "s"(srd), [idx] "v"(idx), [vwo] "v"(vwo), [fvo] "v"(fvo),
          [gd] "s"(P.dly[0]), [gq] "s"(P.dly[1]), [dr] "s"(P.dly[6]), [gx] "s"(P.dly[7]), [th] "s"(P.dly[0] - P.dly[2] - P.dly[12]), [tso] "v"(tso), [tsw] "v"(tsw), [fra] "s"(C_U2F + h * 4),
          [t0] "s"(t0), [w] "s"(w)
        : "v96", "v97", "v98", "v99", CL8(10), CL8(11), CL8(12), CL8(20), CL8(21),
          CL8(22), CL8(23), CL8(24), "v250", "v251", "v252", "v253", "v254", "v255", SCL, "vcc", "scc", "memory");
    if (budget < 0 && lane == 0) st32<SC1>(3, rws, C_ERR, 0);
    STAMP(320 + h);
    PDUMP(64 + h);
}

#define XDMA(o) "buffer_load_dwordx4 %[xo], %[srd], s66 offen offset:" #o " sc1 lds\n"
#define XDMA8 "s_nop 0\n" XDMA(0) XDMA(1024) XDMA(2048) XDMA(3072) "s_add_u32 m0, m0, 0x1000\ns_add_u32 s66, s66, 0x1000\n" XDMA(0) XDMA(1024) XDMA(2048) XDMA(3072)
#define KQ_WISSUE \
    ".set wl_i, 0\n.rept 16\n" \
    "s_mov_b32 s62, (64*wl_t + 32*(wl_i>>3) + (wl_i&7))*8192\n" \
    "buffer_load_dwordx2 v[128+(wl_t&3)*32+wl_i*2:129+(wl_t&3)*32+wl_i*2], %[wvo], %[rw], s62 offen\n" \
    ".set wl_i, wl_i+1\n.endr\n.set wl_t, wl_t+1\n"
#define KQ_CVT(a, b) \
    "v_cvt_pk_bf16_f32 v[112+kq_t*8+kq_h*4+" #a "], v[64+kq_t*16+(" #a "&1)*8+2*kq_h+(" #a ">>1)], v[68+kq_t*16+(" #a "&1)*8+2*kq_h+(" #a ">>1)]\n" \
    "v_cvt_pk_bf16_f32 v[112+kq_t*8+kq_h*4+" #b "], v[64+kq_t*16+(" #b "&1)*8+2*kq_h+(" #b ">>1)], v[68+kq_t*16+(" #b "&1)*8+2*kq_h+(" #b ">>1)]\n"
#define KQ_ST(so) "buffer_store_dwordx4 v[112+kq_t*8+kq_h*4:115+kq_t*8+kq_h*4], %[oo], %[srd], " so " offen offset:kq_h*1024 sc1\n"
#define KQ_MF(b) \
    ".if (kq_n & 15) == 0\n" MF "v[64+(kq_n>>4)*16+" #b "*4:67+(kq_n>>4)*16+" #b "*4], a[" #b "*4:" #b "*4+3], v[96+(kq_n&3)*4:99+(kq_n&3)*4], 0\n.else\n" \
    MF "v[64+(kq_n>>4)*16+" #b "*4:67+(kq_n>>4)*16+" #b "*4], a[(kq_n&15)*16+" #b "*4:(kq_n&15)*16+" #b "*4+3], v[96+(kq_n&3)*4:99+(kq_n&3)*4], v[64+(kq_n>>4)*16+" #b "*4:67+(kq_n>>4)*16+" #b "*4]\n.endif\n"
DEV void role_kq(const Params& P, rsrc_t rws, u32x4 srd, int h, int w, int lane) {
    const int g = lane >> 4, c = lane & 15;
    const u32x4 rw = mksrd(w < 2 ? P.wk : P.wq, 4u << 20);
    const unsigned wvo = 8 * g * 8192 + h * 256 + (64 * (w & 1) + 4 * c) * 2;
    const unsigned xo = OFF_XN + w * 8192 + lane * 16, l16 = lane * 16;
    const unsigned oo = (w < 2 ? OFF_KF : OFF_QF) + h * 8192 + (w & 1) * 2048 + lane * 16;
    const unsigned fo = lane == 0 ? C_KQF + h * 16 + w * 4 : OOR;
    int budget = SPIN_CAP;
    asm volatile(
        "s_mov_b32 s63, 0x05040100\ns_mov_b32 s64, 0x07060302\n"
        ".set wl_t, 0\n.rept 3\n" KQ_WISSUE ".endr\n"
        ".set wl_p, 0\n.rept 8\n"
        ".if wl_p < 6\ns_waitcnt vmcnt(32)\n.elseif wl_p == 6\ns_waitcnt vmcnt(16)\n.else\ns_waitcnt vmcnt(0)\n.endif\n"
        ".if wl_t < 8\n" KQ_WISSUE ".endif\n"
        ".set wl_i, 0\n.rept 8\n"
        ".set wl_b, 128+(wl_p&3)*32+wl_i*4\n.set wl_d, (2*wl_p+(wl_i>>2))*16+(wl_i&3)\n"
        "v_perm_b32 v120, v[wl_b+2], v[wl_b], s63\nv_perm_b32 v121, v[wl_b+2], v[wl_b], s64\n"
        "v_perm_b32 v122, v[wl_b+3], v[wl_b+1], s63\nv_perm_b32 v123, v[wl_b+3], v[wl_b+1], s64\n"
        "v_accvgpr_write_b32 a[wl_d], v120\nv_accvgpr_write_b32 a[wl_d+4], v121\n"
        "v_accvgpr_write_b32 a[wl_d+8], v122\nv_accvgpr_write_b32 a[wl_d+12], v123\n"
        ".set wl_i, wl_i+1\n.endr\n.set wl_p, wl_p+1\n.endr\n"
        CLRWAIT(0)
        "s_lshl_b32 s68, %[w], 13\n"
        "s_mov_b32 m0, s68\ns_mov_b32 s66, 0\n" XDMA8
        "s_add_u32 m0, s68, 0x8000\ns_mov_b32 s66, 0x8000\n" XDMA8
        "s_mov_b32 s69, 0\ns_mov_b32 s70, 0x40000000\ns_mov_b32 s71, 0\n"
        "v_mov_b32 v60, %[l16]\nv_add_u32 v61, 0x8000, %[l16]\n"
        "s_waitcnt vmcnt(8)\ns_barrier\n"
        "ds_read_b128 v[96:99], v60\nds_read_b128 v[100:103], v60 offset:1024\nds_read_b128 v[104:107], v60 offset:2048\n"
        ".p2align 6\nLloop%=:\n"
        ".set kq_n, 0\n.rept 32\n"
        ".if kq_n == 29\ns_barrier\n.endif\n"
        "s_waitcnt lgkmcnt(2)\n"
        KQ_MF(0)
        ".if kq_n < 29\nds_read_b128 v[96+((kq_n+3)&3)*4:99+((kq_n+3)&3)*4], v60 offset:(kq_n+3)*1024\n"
        ".else\nds_read_b128 v[96+((kq_n+3)&3)*4:99+((kq_n+3)&3)*4], v61 offset:(kq_n-29)*1024\n.endif\n"
        KQ_MF(1)
        ".if kq_n == 1 || kq_n == 2\n.set kq_t, 1\n.set kq_h, kq_n-1\n" KQ_CVT(0, 1) ".endif\n"
        ".if kq_n == 18 || kq_n == 19\n.set kq_t, 0\n.set kq_h, kq_n-18\n" KQ_CVT(0, 1) ".endif\n"
        ".if kq_n == 3\ns_add_u32 s66, s69, 2\ns_and_b32 s67, s66, 3\ns_lshl_b32 s67, s67, 15\n.endif\n"
        ".if kq_n == 8\ns_add_u32 m0, m0, 0x1000\ns_add_u32 s66, s66, 0x1000\n.endif\n"
        ".if kq_n == 14\nv_mov_b32 v62, s69\n.endif\n"
        KQ_MF(2)
        ".if kq_n == 1 || kq_n == 2 || kq_n == 18 || kq_n == 19\n" KQ_CVT(2, 3) ".endif\n"
        ".if kq_n == 3\ns_add_u32 m0, s67, s68\ns_lshl_b32 s66, s66, 15\n.endif\n"
        ".if kq_n >= 4 && kq_n < 12\nbuffer_load_dwordx4 %[xo], %[srd], s66 offen offset:(kq_n&3)*1024 sc1 lds\n.endif\n"
        ".if kq_n == 14\ns_waitcnt vmcnt(8)\nbuffer_store_dword v62, %[fo], %[srd], 0 offen sc1\n.endif\n"
        KQ_MF(3)
        ".if kq_n == 1 || kq_n == 2\n" KQ_ST("s70") ".endif\n"
        ".if kq_n == 18 || kq_n == 19\n" KQ_ST("s71") ".endif\n"
        ".if kq_n == 30\ns_add_u32 s70, s71, 0x1000\ns_add_u32 s71, s71, 0x40000\ns_add_u32 s69, s69, 1\n.endif\n"
        ".if kq_n == 31\ns_add_u32 s67, s69, 1\ns_and_b32 s67, s67, 3\ns_lshl_b32 s67, s67, 15\n.endif\n"
        ".set kq_n, kq_n+1\n.endr\n"
        "v_mov_b32 v60, v61\n"
        "v_add_u32 v61, s67, %[l16]\n"
        "s_cmp_lt_u32 s69, 64\n"
        "s_cbranch_scc1 Lloop%=\n"
        "s_nop 7\n"
        ".set kq_t, 1\n.set kq_h, 0\n" KQ_CVT(0, 1) KQ_CVT(2, 3) KQ_ST("s70")
        ".set kq_h, 1\n" KQ_CVT(0, 1) KQ_CVT(2, 3) KQ_ST("s70")
        "v_mov_b32 v62, 64\n"
        "s_waitcnt vmcnt(0) lgkmcnt(0)\n"
        "buffer_store_dword v62, %[fo], %[srd], 0 offen sc1\n"
        : [bud] "+s"(budget)
        : [rw] "s"(rw), [wvo] "v"(wvo), [srd] "s"(srd), [xo] "v"(xo), [l16] "v"(l16), [oo] "v"(oo), [fo] "v"(fo), [w] "s"(w)
        : ACL, CL8(6), CL8(7), CL8(8), CL8(9), CL8(10), CL8(11), CL8(12), CL8(13), CL8(14), CL8(15), CL8(16), CL8(17), CL8(18), CL8(19), CL8(20), CL8(21),
          CL8(22), CL8(23), CL8(24), "v250", "v251", "v252", "v253", "v254", "v255", SCL, "vcc", "scc", "memory");
    if (budget < 0 && lane == 0) st32<SC1>(5, rws, C_ERR, 0);
    STAMP(192 + h);
}

#define VW_VISSUE \
    ".set wl_i, 0\n.rept 16\n" \
    "s_mov_b32 s62, (64*wl_t + 32*(wl_i>>3) + (wl_i&7))*8192\n" \
    "buffer_load_dword v[96+wl_t*16+wl_i], %[wvo], %[rv], s62 offen\n" \
    ".set wl_i, wl_i+1\n.endr\n.set wl_t, wl_t+1\n"
#define VW_CVT(a, b) \
    "v_cvt_pk_bf16_f32 v[224+(vw_i&3)*4+" #a "], v[160+vw_i*8+" #a "], v[164+vw_i*8+" #a "]\n" \
    "v_cvt_pk_bf16_f32 v[224+(vw_i&3)*4+" #b "], v[160+vw_i*8+" #b "], v[164+vw_i*8+" #b "]\n"
#define VW_ST(so) "s_add_u32 s72, " so ", vw_i*0x8000\nbuffer_store_dwordx4 v[224+(vw_i&3)*4:227+(vw_i&3)*4], %[oo], %[srd], s72 offen sc1\n"
#define VW_VC(t) \
    "v_cvt_pk_bf16_f32 v[240+" #t "*4], v[112+" #t "*8], v[116+" #t "*8]\nv_cvt_pk_bf16_f32 v[241+" #t "*4], v[113+" #t "*8], v[117+" #t "*8]\n" \
    "v_cvt_pk_bf16_f32 v[242+" #t "*4], v[114+" #t "*8], v[118+" #t "*8]\nv_cvt_pk_bf16_f32 v[243+" #t "*4], v[115+" #t "*8], v[119+" #t "*8]\n" \
    "ds_write_b128 v62, v[240+" #t "*4:243+" #t "*4] offset:" #t "*4096\n"
#define VW_MV(b) \
    ".if (vw_n & 15) == 0\n" MF "v[112+(vw_n>>4)*8+" #b "*4:115+(vw_n>>4)*8+" #b "*4], a[" #b "*4:" #b "*4+3], v[96+(vw_n&3)*4:99+(vw_n&3)*4], 0\n.else\n" \
    MF "v[112+(vw_n>>4)*8+" #b "*4:115+(vw_n>>4)*8+" #b "*4], a[(vw_n&15)*8+" #b "*4:(vw_n&15)*8+" #b "*4+3], v[96+(vw_n&3)*4:99+(vw_n&3)*4], v[112+(vw_n>>4)*8+" #b "*4:115+(vw_n>>4)*8+" #b "*4]\n.endif\n"
#define VW_MO \
    ".if vw_k == 0\n" MF "v[160+vw_a*8+vw_t*4:163+vw_a*8+vw_t*4], v[128+vw_t*4:131+vw_t*4], a[128+vw_a*16:131+vw_a*16], 0\n.else\n" \
    MF "v[160+vw_a*8+vw_t*4:163+vw_a*8+vw_t*4], v[128+vw_k*8+vw_t*4:131+vw_k*8+vw_t*4], a[128+vw_a*16+vw_k*4:131+vw_a*16+vw_k*4], v[160+vw_a*8+vw_t*4:163+vw_a*8+vw_t*4]\n.endif\n"
#define VW_VF(k) "ds_read_b128 v[128+" #k "*8:131+" #k "*8], v63 offset:" #k "*1024\nds_read_b128 v[132+" #k "*8:135+" #k "*8], v63 offset:4096+" #k "*1024\n"
#define VW_BODY \
    ".set vw_n, 0\n.rept 32\n" \
    ".if vw_n < 3\ns_waitcnt lgkmcnt(4)\n.elseif vw_n < 30\ns_waitcnt lgkmcnt(2)\n.elseif vw_n == 30\ns_waitcnt lgkmcnt(1)\n.else\ns_waitcnt lgkmcnt(0)\n.endif\n" \
    VW_MV(0) \
    ".if vw_n < 29\nds_read_b128 v[96+((vw_n+3)&3)*4:99+((vw_n+3)&3)*4], v60 offset:(vw_n+3)*1024\n.endif\n" \
    ".if vw_f == 0\n" \
    ".if vw_n >= 1 && vw_n <= 8\n.set vw_i, 4+((vw_n-1)>>1)\n.elseif vw_n == 10 || vw_n == 11\n.set vw_i, 0\n.elseif vw_n == 18 || vw_n == 19\n.set vw_i, 1\n" \
    ".elseif vw_n == 26 || vw_n == 27\n.set vw_i, 2\n.else\n.set vw_i, -1\n.endif\n" \
    ".if vw_n <= 8\n.set vw_e, 1-(vw_n&1)\n.else\n.set vw_e, vw_n&1\n.endif\n" \
    ".if vw_i >= 0 && vw_e == 0\n" VW_CVT(0, 1) ".endif\n" \
    ".set vw_a, vw_n>>3\n.set vw_k, (vw_n>>1)&3\n.set vw_t, vw_n&1\n" VW_MO \
    ".if vw_i >= 0 && vw_e == 0\n" VW_CVT(2, 3) ".endif\n" \
    ".if vw_i >= 4 && vw_e != 0\n" VW_ST("s70") ".endif\n" \
    ".if vw_i >= 0 && vw_i < 4 && vw_e != 0\n" VW_ST("s71") ".endif\n" \
    ".if vw_n == 21\nv_mov_b32 v248, s69\ns_waitcnt vmcnt(10)\nbuffer_store_dword v248, %[fo], %[srd], 0 offen sc1\n.endif\n" \
    ".endif\n" \
    ".if vw_n == 9\ns_mov_b32 m0, s73\n.endif\n" \
    ".if vw_n == 16\ns_add_u32 m0, m0, 0x1000\ns_add_u32 s66, s66, 0x1000\n.endif\n" \
    VW_MV(1) \
    ".if vw_n >= 12 && vw_n < 20\nbuffer_load_dwordx4 %[xo], %[srd], s66 offen offset:(vw_n&3)*1024 sc1 lds\n.endif\n" \
    ".if vw_n == 18\n" VW_VC(0) ".endif\n" \
    ".set vw_n, vw_n+1\n.endr\n" \
    ".set vw_n, 0\n.rept 32\n" \
    ".set vw_a, 4+((vw_n>>1)&3)\n.set vw_k, vw_n>>3\n.set vw_t, vw_n&1\n" \
    ".if vw_f == 0\n" VW_MO ".endif\n" \
    ".if vw_n == 1\ns_nop 5+2*vw_f\n.endif\n" \
    ".if vw_n == 2\n" VW_VC(1) ".endif\n" \
    ".if vw_n == 3 && vw_f == 0\n.set vw_i, 3\n" VW_CVT(0, 1) VW_CVT(2, 3) ".endif\n" \
    ".if vw_n == 4 && vw_f == 0\n" VW_ST("s71") ".endif\n" \
    ".if vw_n == 5\n.if vw_f\ns_waitcnt vmcnt(8)\n.endif\ns_waitcnt lgkmcnt(0)\ns_barrier\n.endif\n" \
    ".if vw_n == 8\n" VW_VF(0) ".endif\n.if vw_n == 16\n" VW_VF(1) ".endif\n.if vw_n == 24\n" VW_VF(2) ".endif\n" \
    ".if vw_n == 10\ns_mov_b32 s70, s71\ns_add_u32 s71, s71, 0x100000\ns_add_u32 s69, s69, 1\n.endif\n" \
    ".if vw_n == 11\ns_mov_b32 s73, s74\ns_mov_b32 s74, s75\ns_sub_u32 s75, m0, 0x1000\n.endif\n" \
    ".if vw_n == 12\ns_add_u32 s66, s66, 0x7000\ns_sub_u32 s67, s74, s68\n.endif\n" \
    ".if vw_n == 13\nv_add_u32 v60, s67, %[l16]\nv_xor_b32 v62, 0x2000, v62\n.endif\n" \
    ".if vw_n == 26 || vw_n == 28 || vw_n == 30\nds_read_b128 v[96+((vw_n-26)>>1)*4:99+((vw_n-26)>>1)*4], v60 offset:((vw_n-26)>>1)*1024\n.endif\n" \
    ".set vw_n, vw_n+1\n.endr\n" \
    VW_VF(3) \
    "v_xor_b32 v63, 0x2000, v63\n"
DEV void role_vw(const Params& P, rsrc_t rws, u32x4 srd, int h, int w, int lane) {
    const int g = lane >> 4, c = lane & 15;
    const u32x4 rv = mksrd(P.wv, 4u << 20), ro = mksrd(P.wo, 4u << 20);
    const unsigned wvo = 8 * g * 8192 + h * 256 + (32 * w + 2 * c) * 2;
    const unsigned woo = (h * 128 + 8 * g) * 1024 + (128 * w + 8 * c) * 2;
    const unsigned xo = OFF_XN + w * 8192 + lane * 16, l16 = lane * 16;
    const unsigned oo = OFF_VW + (8 * w * 32 + h) * 1024 + lane * 16;
    const unsigned fo = lane == 0 ? C_VWF + w * 128 + h * 4 : OOR;
    int budget = SPIN_CAP;
    asm volatile(
        "s_mov_b32 s63, 0x05040100\ns_mov_b32 s64, 0x07060302\n"
        ".set wl_i, 0\n.rept 32\n"
        "s_mov_b32 s62, (32*(wl_i>>3) + (wl_i&4))*1024\n"
        "buffer_load_dwordx4 v[128+wl_i*4:131+wl_i*4], %[woo], %[ro], s62 offen offset:(wl_i&3)*1024\n"
        ".set wl_i, wl_i+1\n.endr\n"
        ".set wl_t, 0\n" VW_VISSUE
        ".set wl_p, 0\n.rept 2\n"
        "s_waitcnt vmcnt(32)\n" VW_VISSUE
        ".set wl_i, 0\n.rept 8\n.set wl_k, 0\n.rept 4\n"
        ".set wl_b, 128+wl_p*64+wl_i*8+wl_k\n.set wl_d, 128+wl_k*32+wl_p*8+wl_i\n"
        "v_perm_b32 v84, v[wl_b+4], v[wl_b], s63\nv_perm_b32 v85, v[wl_b+4], v[wl_b], s64\n"
        "v_accvgpr_write_b32 a[wl_d], v84\nv_accvgpr_write_b32 a[wl_d+16], v85\n"
        ".set wl_k, wl_k+1\n.endr\n.set wl_i, wl_i+1\n.endr\n"
        ".set wl_p, wl_p+1\n.endr\n"
        ".set wl_p, 0\n.rept 8\n"
        ".if wl_p < 6\ns_waitcnt vmcnt(32)\n.elseif wl_p == 6\ns_waitcnt vmcnt(16)\n.else\ns_waitcnt vmcnt(0)\n.endif\n"
        ".if wl_t < 8\n" VW_VISSUE ".endif\n"
        ".set wl_i, 0\n.rept 8\n"
        ".set wl_b, 96+wl_p*16+wl_i*2\n.set wl_d, wl_p*16+(wl_i>>2)*8+(wl_i&3)\n"
        "v_perm_b32 v84, v[wl_b+1], v[wl_b], s63\nv_perm_b32 v85, v[wl_b+1], v[wl_b], s64\n"
        "v_accvgpr_write_b32 a[wl_d], v84\nv_accvgpr_write_b32 a[wl_d+4], v85\n"
        ".set wl_i, wl_i+1\n.endr\n.set wl_p, wl_p+1\n.endr\n"
        CLRWAIT(0)
        "s_lshl_b32 s68, %[w], 13\n"
        "s_mov_b32 m0, s68\ns_mov_b32 s66, 0\n" XDMA8
        "s_add_u32 m0, s68, 0x8000\ns_mov_b32 s66, 0x8000\n" XDMA8
        "s_add_u32 s73, s68, 0x10000\ns_mov_b32 s74, s68\ns_add_u32 s75, s68, 0x8000\ns_mov_b32 s66, 0x10000\n"
        "s_mov_b32 s69, -1\ns_mov_b32 s71, -0x100000\n"
        "s_lshl_b32 s67, %[w], 10\ns_add_u32 s67, s67, 0x18000\n"
        "v_mov_b32 v60, %[l16]\nv_add_u32 v62, s67, %[l16]\nv_add_u32 v63, 0x18000, %[l16]\n"
        "s_waitcnt vmcnt(8)\ns_barrier\n"
        "ds_read_b128 v[96:99], v60\nds_read_b128 v[100:103], v60 offset:1024\nds_read_b128 v[104:107], v60 offset:2048\n"
        "s_waitcnt lgkmcnt(0)\n"
        ".set vw_f, 1\n" VW_BODY
        "s_mov_b32 s70, 0x40000000\n"
        ".p2align 6\nLloop%=:\n"
        ".set vw_f, 0\n" VW_BODY
        "s_cmp_lt_u32 s69, 64\n"
        "s_cbranch_scc1 Lloop%=\n"
        "s_nop 7\n"
        ".set vw_i, 4\n.rept 4\n" VW_CVT(0, 1) VW_CVT(2, 3) VW_ST("s70") ".set vw_i, vw_i+1\n.endr\n"
        "v_mov_b32 v248, 64\n"
        "s_waitcnt vmcnt(0) lgkmcnt(0)\n"
        "buffer_store_dword v248, %[fo], %[srd], 0 offen sc1\n"
        : [bud] "+s"(budget)
        : [rv] "s"(rv), [ro] "s"(ro), [wvo] "v"(wvo), [woo] "v"(woo), [srd] "s"(srd), [xo] "v"(xo), [l16] "v"(l16), [oo] "v"(oo), [fo] "v"(fo), [w] "s"(w)
        : ACL, CL8(6), CL8(7), CL8(8), CL8(9), CL8(10), CL8(11), CL8(12), CL8(13), CL8(14), CL8(15), CL8(16), CL8(17), CL8(18), CL8(19), CL8(20), CL8(21),
          CL8(22), CL8(23), CL8(24), "v250", "v251", "v252", "v253", "v254", "v255", SCL, "vcc", "scc", "memory");
    if (budget < 0 && lane == 0) st32<SC1>(6, rws, C_ERR, 0);
    STAMP(224 + h);
}

struct TrueT { static constexpr bool value = true; };
struct FalseT { static constexpr bool value = false; };
DEV float bfr(float v) { return (float)(__bf16)v; }
DEV u32x2 next_tag(u32x2 e) {
    asm volatile("" : "+v"(e));
    const unsigned long long t = (((unsigned long long)(unsigned)RFL(e.y) << 32) | (unsigned)RFL(e.x)) + 2;
    return u32x2{(unsigned)t, (unsigned)(t >> 32)};
}
DEV unsigned wait_tags(rsrc_t rws, unsigned off, u32x2 tag, int& budget) {
    u32x2 v;
#pragma nounroll
    for (;;) {
        v = ld64<SC1>(rws, off, 0);
        if (__all((v.x | 1u) == (tag.x | 1u) && v.y == tag.y) || --budget < 0) break;
        __builtin_amdgcn_s_sleep(4);
    }
    asm volatile("" ::: "memory");
    return v.x & 1u;
}

DEV void role_ffn(const Params& P, rsrc_t rws, char* lds, int s, int w, int lane, u32x2 ep, int& budget) {
    const int g = lane >> 4, c = lane & 15;
    u32x2 tag;
    const rsrc_t rx = mkrs(P.x, 2048 * 512 * 2);
    {
        const int r8 = lane & 7, q = lane >> 3;
        u32x4 xv[8];
#pragma unroll
        for (int i = 0; i < 8; ++i) xv[i] = ld128<0>(rx, (32 * s + 8 * w + r8) * 1024 + q * 16 + i * 128, 0);
        f32x2 v[32];
#pragma unroll
        for (int i = 0; i < 8; ++i)
#pragma unroll
            for (int k = 0; k < 4; ++k) v[4 * i + k] = f32x2{asf(xv[i][k] << 16), asf(xv[i][k] & 0xFFFF0000u)};
        auto rowred = [](float a) { a += dppf<0x128>(a); a = add16(a); return fold32(a, a); };
        f32x2 sm = v[0];
#pragma unroll
        for (int k = 1; k < 32; ++k) sm += v[k];
        const float mean = rowred(sm.x + sm.y) * (1.0f / 512.0f);
        f32x2 sq = {0.f, 0.f};
#pragma unroll
        for (int k = 0; k < 32; ++k) { const f32x2 d = v[k] - mean; sq = __builtin_elementwise_fma(d, d, sq); }
        const float var = bfr(rowred(sq.x + sq.y) * (1.0f / 512.0f));
        const float mb = bfr(mean);
        const float den = bfr(sqrtf(bfr(var + 1e-5f)));
        const float rc = 1.0f / den;
        const unsigned dst = (w >> 1) * 16384 + (q >> 2) * 1024 + ((q & 3) * 16 + (w & 1) * 8 + r8) * 16;
#pragma unroll
        for (int i = 0; i < 8; ++i) {
            u32x4 o;
#pragma unroll
            for (int k = 0; k < 4; ++k) {
                const f32x2 d = v[4 * i + k] - mb;
                const unsigned db = pk(d.x, d.y);
                const f32x2 a = {asf(db << 16), asf(db & 0xFFFF0000u)};
                f32x2 qv = a * rc;
                const f32x2 rem = __builtin_elementwise_fma(-qv, f32x2{den, den}, a);
                qv = __builtin_elementwise_fma(rem, f32x2{rc, rc}, qv);
                o[k] = pk(qv.x, qv.y);
            }
            st128<SC1>(o, rws, OFF_XN + dst + i * 2048, s * 32768);
            *(u32x4*)(lds + dst + i * 2048) = o;
        }
        if (threadIdx.x < 8) st32<SC1>(0, rws, OFF_CTRL + 1024 + s * 32 + threadIdx.x * 4, 0);
        tag = next_tag(ep);
        WAIT_VM0();
        BAR();
        if (threadIdx.x == 0) st64<SC1>(u32x2{tag.x | 1u, tag.y}, rws, C_XF + s * 8, 0);
    }
    STAMP(960 + s);
    {
        const unsigned t1 = (unsigned)__builtin_amdgcn_s_memrealtime() + P.dly[9];
        while ((int)(t1 - (unsigned)__builtin_amdgcn_s_memrealtime()) > 0) __builtin_amdgcn_s_sleep(8);
    }
    const rsrc_t r1 = mkrs(P.w1, 512 * 1376 * 2), r2 = mkrs(P.w2, 1376 * 512 * 2);
    const unsigned vo2 = 32 * g * 1024 + (128 * w + 8 * c) * 2;
    u32x4 raw[4][8];
    f32x4 acc[8][2];
    const int rot = s >> 2, rot2 = (rot * 11) >> 2;
    auto issue1 = [&](int u, int sl) {
        const unsigned col = (w + 4 * (u >> 4)) * 128 + 8 * c;
        const unsigned vo = col < 1376 ? 8 * g * 2752 + col * 2 : OOR;
#pragma unroll
        for (int j = 0; j < 8; ++j) raw[sl][j] = ld128<0>(r1, vo, (((u + rot) & 15) * 32 + j) * 2752);
    };
    auto issue2 = [&](int k0, int sl) {
        const int k = k0 < 44 ? (k0 + rot2 < 44 ? k0 + rot2 : k0 + rot2 - 44) : 44;
#pragma unroll
        for (int j = 0; j < 8; ++j) raw[sl][j] = ld128<0>(r2, vo2 + (j & 3) * 1024, ((k >> 2) * 128 + (k & 3) * 8 + (j & 4)) * 1024);
    };
    auto frags = [&](int sl, u32x4 (&F)[8]) {
#pragma unroll
        for (int jj = 0; jj < 4; ++jj)
#pragma unroll
            for (int i = 0; i < 4; ++i) { F[2 * i][jj] = plo(raw[sl][2 * jj][i], raw[sl][2 * jj + 1][i]); F[2 * i + 1][jj] = phi(raw[sl][2 * jj][i], raw[sl][2 * jj + 1][i]); }
    };
    auto zero = [&]() {
#pragma unroll
        for (int i = 0; i < 8; ++i) { acc[i][0] = f32x4{0, 0, 0, 0}; acc[i][1] = f32x4{0, 0, 0, 0}; }
    };
    const int nst = w < 3 ? 48 : 32;
#pragma unroll
    for (int i = 0; i < 4; ++i) issue1(i, i);
    zero();
    auto group = [&](int u, auto last) {
#pragma unroll
        for (int i = 0; i < 4; ++i) {
            SCHED();
            const u32x4 x0 = *(const u32x4*)(lds + ((u + i + rot) & 15) * 1024 + lane * 16), x1 = *(const u32x4*)(lds + 16384 + ((u + i + rot) & 15) * 1024 + lane * 16);
            u32x4 F[8];
            frags(i, F);
            if constexpr (decltype(last)::value) issue2(i, i); else issue1(u + 4 + i, i);
#pragma unroll
            for (int n = 0; n < 8; ++n) { acc[n][0] = mfma(F[n], x0, acc[n][0]); acc[n][1] = mfma(F[n], x1, acc[n][1]); }
        }
    };
    auto hidden = [&](int u) {
        char* hb = lds + 32768 + (w + 4 * (u >> 4)) * 8192 + lane * 16;
#pragma unroll
        for (int r = 0; r < 4; ++r)
#pragma unroll
            for (int t = 0; t < 2; ++t) {
                u32x4 d;
#pragma unroll
                for (int n = 0; n < 4; ++n) d[n] = pk(fmaxf(acc[2 * n][t][r], 0.f), fmaxf(acc[2 * n + 1][t][r], 0.f));
                *(u32x4*)(hb + r * 2048 + t * 1024) = d;
            }
        zero();
    };
#pragma nounroll
    for (int u = 0; u < nst - 4; u += 4) {
        group(u, FalseT());
        if ((u & 15) == 12) hidden(u);
    }
    group(nst - 4, TrueT());
    hidden(nst - 4);
    BAR();
    STAMP(832 + s);
#pragma nounroll
    for (int k = 0; k < 44; k += 4) {
#pragma unroll
        for (int i = 0; i < 4; ++i) {
            SCHED();
            const int kr = k + i + rot2 < 44 ? k + i + rot2 : k + i + rot2 - 44;
            const u32x4 h0 = *(const u32x4*)(lds + 32768 + kr * 2048 + lane * 16), h1 = *(const u32x4*)(lds + 33792 + kr * 2048 + lane * 16);
            u32x4 F[8];
            frags(i, F);
            issue2(k + 4 + i, i);
#pragma unroll
            for (int n = 0; n < 8; ++n) { acc[n][0] = mfma(h0, F[n], acc[n][0]); acc[n][1] = mfma(h1, F[n], acc[n][1]); }
        }
    }
#pragma unroll
    for (int t = 0; t < 2; ++t)
#pragma unroll
        for (int r = 0; r < 4; ++r) {
            const unsigned tok = 32 * s + 16 * t + 4 * g + r;
            const u32x4 xv = ld128<0>(rx, tok * 1024 + (128 * w + 8 * c) * 2, 0);
            u32x4 o0, o1;
#pragma unroll
            for (int i = 0; i < 2; ++i) {
                o0[2 * i] = asu(acc[2 * i][t][r] + asf(xv[i] << 16)); o0[2 * i + 1] = asu(acc[2 * i + 1][t][r] + asf(xv[i] & 0xFFFF0000u));
                o1[2 * i] = asu(acc[4 + 2 * i][t][r] + asf(xv[2 + i] << 16)); o1[2 * i + 1] = asu(acc[5 + 2 * i][t][r] + asf(xv[2 + i] & 0xFFFF0000u));
            }
            st128<SC1>(o0, rws, OFF_BASE + tok * 2048 + (128 * w + 8 * c) * 4, 0);
            st128<SC1>(o1, rws, OFF_BASE + tok * 2048 + (128 * w + 8 * c) * 4 + 16, 0);
        }
    wait_tags(rws, C_XF + lane * 8, tag, budget);
    WAIT_VM0();
    BAR();
    if (threadIdx.x == 0) st32<SC1>(1, rws, C_BF + s * 4, 0);
    STAMP(256 + s);
}

DEV void role_out(const Params& P, rsrc_t rws, char* lds, int id, int w, int lane, u32x2 ep, int& budget) {
    const int g = lane >> 4, c = lane & 15;
    const int set = id >> 4, tt = (id >> 3) & 1, cg = id & 7;
    const int j0 = 8 * (cg >> 1) + 4 * (cg & 1);
    const int col = 128 * (cg >> 1) + 8 * c + 4 * (cg & 1);
    unsigned seen = 0;
    const rsrc_t ro = mkrs(P.out, 2048 * 512 * 2);
    const u32x2 tag = next_tag(ep);
    const unsigned votes = (unsigned)__ballot(wait_tags(rws, C_MSG + (lane & 31) * 8, tag, budget) != 0);
    const unsigned h2 = __builtin_popcount(votes & 0x24924924u) > 4 ? 8192 : 0;
    const unsigned fo = C_U2F + lane * 4;
    const int smax = lane < 32 ? 59 : 63;
#pragma nounroll
    for (;;) {
        const unsigned v = ld32<SC1>(rws, C_BF + lane * 4, 0);
        if (__all(v == 1u) || --budget < 0) break;
        __builtin_amdgcn_s_sleep(16);
    }
    asm volatile("" ::: "memory");
#pragma nounroll
    for (int s = set; s < NSEG; s += 4) {
        if (seen <= (unsigned)s) seen = wait_flags<8>(rws, C_VWF + (cg >> 1) * 128 + w * 32, s, budget, lane);
        u32x4 VW[4][8];
#pragma unroll
        for (int i = 0; i < 4; ++i)
#pragma unroll
            for (int hh = 0; hh < 8; ++hh)
                VW[i][hh] = ld128<SC1>(rws, OFF_VW + ((j0 + i) * 32 + 8 * w + (hh & 4)) * 1024 + lane * 16 + (hh & 3) * 1024, s * MB);
        const unsigned tok = 32 * s + 16 * tt + 4 * g + w;
        const u32x4 bs = ld128<SC1>(rws, OFF_BASE + tok * 2048 + col * 4, 0);
        if (s >= 60) STAMP(704 + id);
        if (w == 0) {
#pragma nounroll
            for (;;) {
                const unsigned v = ld32<SC1>(rws, fo, 0);
                if (__all(v > (unsigned)min(s, smax)) || --budget < 0) break;
                if (s >= 62 && __all(v >= min(62u, (unsigned)smax))) __builtin_amdgcn_s_sleep(3);
                else __builtin_amdgcn_s_sleep(36);
            }
        }
        BAR();
        if (s >= 60) STAMP(768 + id);
        const unsigned uo = s < 60 ? OFF_U2 + s * 262144 + j0 * 8192 : OFF_URING + h2 + (s & 3) * 524288 + j0 * 16384;
        const unsigned us = s < 60 ? 8192 : 16384;
        u32x4 U[4][2];
#define ULD(i) U[i][0] = ld128<SC1>(rws, 2 * w * 1024 + lane * 16, uo + i * us); U[i][1] = ld128<SC1>(rws, 2 * w * 1024 + 1024 + lane * 16, uo + i * us);
        ULD(0) ULD(1) ULD(2) ULD(3)
        u32x4 Px[8], Pm[2];
#pragma unroll
        for (int hh = 0; hh < 8; ++hh) Px[hh] = ld128<SC1>(rws, OFF_P + tt * 40960 + (8 * w + (hh & 4)) * 1024 + lane * 16 + (hh & 3) * 1024, s * 81920);
#pragma unroll
        for (int n = 0; n < 2; ++n) Pm[n] = ld128<SC1>(rws, OFF_P + tt * 40960 + (32 + 2 * w + n) * 1024 + lane * 16, s * 81920);
        f32x4 acc[4];
#pragma unroll
        for (int i = 0; i < 4; ++i) acc[i] = f32x4{0, 0, 0, 0};
#pragma unroll
        for (int hh = 0; hh < 8; ++hh)
#pragma unroll
            for (int i = 0; i < 4; ++i) acc[i] = mfma(Px[hh], VW[i][hh], acc[i]);
        if (s >= 60) {
#pragma nounroll
            for (;;) {
                const unsigned m = max(max(max(umax4(U[0][0]), umax4(U[0][1])), max(umax4(U[1][0]), umax4(U[1][1]))),
                                       max(max(umax4(U[2][0]), umax4(U[2][1])), max(umax4(U[3][0]), umax4(U[3][1]))));
                if (__all(m != ~0u) || --budget < 0) break;
                ULD(0) ULD(1) ULD(2) ULD(3)
            }
        }
#pragma unroll
        for (int n = 0; n < 2; ++n)
#pragma unroll
            for (int i = 0; i < 4; ++i) acc[i] = mfma(Pm[n], U[i][n], acc[i]);
#pragma unroll
        for (int v = 0; v < 4; ++v) *(f32x4*)(lds + (w * 4 + v) * 1024 + lane * 16) = f32x4{acc[0][v], acc[1][v], acc[2][v], acc[3][v]};
        BAR();
        f32x4 t = __builtin_bit_cast(f32x4, bs);
#pragma unroll
        for (int o = 0; o < 4; ++o) t += *(const f32x4*)(lds + (o * 4 + w) * 1024 + lane * 16);
        st64<0>(u32x2{pk(t[0], t[1]), pk(t[2], t[3])}, ro, tok * 1024 + col * 2, 0);
    }
    if (id == 63 && threadIdx.x == 0) st64<SC1>(tag, rws, C_EPOCH, 0);
    if (budget < 0 && lane == 0) st32<SC1>(4, rws, C_ERR, 0);
    STAMP(640 + id);
}

DEV void role_cl(const Params& P, rsrc_t rws, u32x4 srd, char* lds, int id, int w, int lane) {
    const int tid = w * 64 + lane;
    int budget = SPIN_CAP;
    const u32x2 ep = ld64<SC1>(rws, C_EPOCH, 0);
    const u32x4 sent = {~0u, ~0u, ~0u, ~0u};
    u32x2 tag;
    unsigned h0, h1, h2;
    auto halves = [&](unsigned off) {
        const unsigned votes = (unsigned)__ballot(wait_tags(rws, off + (lane & 31) * 8, tag, budget) != 0);
        h0 = __builtin_popcount(votes & 0x49249249u) > 5 ? 8192 : 0; h1 = __builtin_popcount(votes & 0x92492492u) > 5 ? 8192 : 0;
        h2 = __builtin_popcount(votes & 0x24924924u) > 4 ? 8192 : 0;
    };
    if (P.rolemask & 2) {
    unsigned m0v;
    {
        const unsigned short* m0 = (const unsigned short*)P.mem0;
        const int r = (lane & 31) >> 2, c0 = 8 * (lane >> 5) + 2 * (lane & 3);
        const int col = 128 * (id >> 3) + 8 * c0 + (id & 7);
        m0v = nosent((unsigned)m0[r * 512 + col] | ((unsigned)m0[r * 512 + col + 8] << 16));
    }
    {
        int best[2] = {1 << 30, 1 << 30};
        const unsigned reg = (id % 3) * 2 * MB + (id / 3) * 65536 + 16384 + w * 2048 + lane * 4;
#pragma nounroll
        for (int i = 0; i < 4; ++i) {
            const long long t0 = __builtin_amdgcn_s_memtime();
            st32<SC1>(~0u, rws, reg + (i & 1) * 8192 + (i >> 1) * 256, 0);
            WAIT_VM0();
            const int dt = (int)(__builtin_amdgcn_s_memtime() - t0);
            if (i & 1) best[1] = min(best[1], dt); else best[0] = min(best[0], dt);
        }
        if (lane == 0) *(u32x2*)(lds + w * 8) = u32x2{(unsigned)best[0], (unsigned)best[1]};
    }
    BAR();
    tag = next_tag(ep);
    u32x2 msg;
    {
        const u32x4 a = *(const u32x4*)lds, b = *(const u32x4*)(lds + 16);
        const unsigned b0 = min(min(a.x, a.z), min(b.x, b.z)), b1 = min(min(a.y, a.w), min(b.y, b.w));
        msg = u32x2{(tag.x & ~1u) | (b0 < b1 ? 0u : 1u), tag.y};
    }
    if (tid == 0) st64<SC1>(msg, rws, C_VOTE + id * 8, 0);
    STAMP(64 + id);
    halves(C_VOTE);
    if (w == 0) st32<SC1>(m0v, rws, OFF_COMM + h0 + id * 256 + lane * 4, 0);
#pragma unroll
    for (int i = 0; i < 8; ++i) st128<SC1>(sent, rws, OFF_URING + h2 + id * 65536 + tid * 16, (i >> 1) * 16384 + (i & 1) * 4096);
#pragma unroll
    for (int i = 0; i < 16; ++i)
        st128<SC1>(sent, rws, (i < 2 && id == 0) ? OOR : OFF_COMM + (id < 16 ? h0 : h1) + id * 131072 + tid * 16, (i >> 1) * 16384 + (i & 1) * 4096);
    STAMP(96 + id);
    WAIT_VM0();
    BAR();
    if (tid == 0) st64<SC1>(msg, rws, C_MSG + id * 8, 0);
    STAMP(352 + id);
    }
    if (!(P.rolemask & 1)) return;
    tag = next_tag(ep);
    halves(C_MSG);
    wait_tags(rws, C_XF + lane * 8, tag, budget);
    const unsigned fo = C_VWF + (id >> 3) * 128 + (lane & 31) * 4;
    const unsigned vo = OFF_VW + id * 32768 + w * 8192 + lane * 16;
    unsigned T = (unsigned)__builtin_amdgcn_s_memrealtime() - P.dly[11];
    bool all = false, ok = false;
#pragma nounroll
    for (int i = 0; i < 60; ++i) {
        while ((int)(T + P.dly[11] - (unsigned)__builtin_amdgcn_s_memrealtime()) > 0) __builtin_amdgcn_s_sleep(1);
        if (P.dly[10] && ok) {
#pragma unroll
            for (int k = 0; k < 8; ++k) {
                const unsigned a = vo + (i + 2) * MB + (k >> 2) * 4096;
                asm volatile("s_mov_b32 m0, %2\ns_nop 0\nbuffer_load_dwordx4 %0, %1, 0 offen offset:%3 sc1 lds" :: "v"(a), "s"(srd), "s"(1024 + w * 4096), "i"((k & 3) * 1024) : "memory");
            }
            while ((int)(T + P.dly[11] + P.dly[10] - (unsigned)__builtin_amdgcn_s_memrealtime()) > 0) __builtin_amdgcn_s_sleep(1);
        }
        const unsigned ts = OFF_COMM + (i + 1) * 65536 + (i + 1 < 32 ? h0 : h1) + 3 * 16384 + 4096 + id * 128;
        unsigned v;
        for (;;) {
            v = RFL(ld32<SC1>(rws, ts, 0));
            if (v != ~0u || --budget < 0) break;
            __builtin_amdgcn_s_sleep(12);
        }
        asm volatile("" ::: "memory");
        const unsigned ro = OFF_URING + h2 + (i & 3) * 524288 + id * 16384 + tid * 16;
        const u32x4 u0 = ld128<SC1>(rws, ro, 0), u1 = ld128<SC1>(rws, ro, 4096);
        T += min((int)(v - T), 0x400);
        st128<SC1>(u0, rws, OFF_U2 + id * 8192 + tid * 16, i * 262144); st128<SC1>(u1, rws, OFF_U2 + id * 8192 + tid * 16 + 4096, i * 262144);
        st128<SC1>(sent, rws, ro, 0); st128<SC1>(sent, rws, ro, 4096);
        if (!all) { const unsigned f = ld32<SC1>(rws, fo, 0); all = __all(f >= NSEG); ok = __all(f > (unsigned)(i + 3)); }
        WAIT_VM0();
        BAR();
        if (tid == 0) st32<SC1>(i + 1, rws, C_U2F + id * 4, 0);
    }
    if (budget < 0 && lane == 0) st32<SC1>(7, rws, C_ERR, 0);
}

__global__ void __launch_bounds__(256, 1) mega_kernel(Params P) {
    extern __shared__ __attribute__((aligned(16))) char lds[];
    const rsrc_t rws = mkrs(P.ws, WS_END);
    const int lane = threadIdx.x & 63;
    const int w = __builtin_amdgcn_readfirstlane(threadIdx.x >> 6);
    const unsigned vb = blockIdx.x, xcd = vb & 7, slot = vb >> 3;
    const u32x4 srd = mksrd(P.ws, WS_END);
    if (xcd < 4) {
        const int id = xcd * 8 + (slot & 7);
        if (slot < 8) { if (P.rolemask & 1) role_qk(P, rws, srd, lds, id, w, lane); }
        else if (slot < 16) { if (P.rolemask & 1) role_vo(P, rws, srd, lds, id, w, lane); }
        else if (slot < 24) { if (P.rolemask & 1) role_ow(P, rws, srd, lds, id, w, lane); }
        else { if (P.rolemask & 3) role_cl(P, rws, srd, lds, id, w, lane); }
    } else {
        const int id = (xcd - 4) * 8 + (slot & 7);
        if (slot < 8) { if (P.rolemask & 4) role_kq(P, rws, srd, id, w, lane); }
        else if (slot < 16) { if (P.rolemask & 16) role_vw(P, rws, srd, id, w, lane); }
        else {
            int budget = SPIN_CAP;
            const u32x2 tag = ld64<SC1>(rws, C_EPOCH, 0);
            if (P.rolemask & 2) role_ffn(P, rws, lds, (slot - 16) * 4 + (xcd - 4), w, lane, tag, budget);
            if (P.rolemask & 8) role_out(P, rws, lds, (xcd - 4) * 16 + (slot - 16), w, lane, tag, budget);
        }
    }
}

extern "C" void kernel_launch(void* const* d_in, const int* in_sizes, int n_in, void* d_out, int out_size, void* d_ws, size_t ws_size,
                              hipStream_t stream) {
    Params P;
    P.x = d_in[0]; P.mem0 = d_in[1]; P.wq = d_in[2]; P.wk = d_in[3]; P.wv = d_in[4]; P.wo = d_in[5];
    P.w1 = d_in[8]; P.w2 = d_in[9];
    P.out = d_out;
    P.ws = (unsigned char*)(((uintptr_t)d_ws + (2 * MB - 1)) & ~(uintptr_t)(2 * MB - 1));
    P.rolemask = 31;
    P.dly[0] = DLY_QK_M; P.dly[1] = DLY_QK_K; P.dly[2] = DLY_VO_U; P.dly[3] = DLY_VO_P; P.dly[4] = DLY_VO_M; P.dly[5] = DLY_MP; P.dly[6] = DLY_PP; P.dly[7] = DLY_X; P.dly[8] = DLY_Q2; P.dly[9] = DLY_FFN; P.dly[10] = DLY_PRE; P.dly[11] = DLY_PRS; P.dly[12] = DLY_PACE;
    (void)hipFuncSetAttribute((const void*)mega_kernel, hipFuncAttributeMaxDynamicSharedMemorySize, LDS_BYTES);
    mega_kernel<<<256, 256, LDS_BYTES, stream>>>(P);
}
```

```cpp
#include <hip/hip_runtime.h>
#include <hip/hip_bf16.h>
#include <stdint.h>

typedef __bf16 bf16x2 __attribute__((ext_vector_type(2)));
typedef __bf16 bf16x8 __attribute__((ext_vector_type(8)));
typedef __bf16 bf16x16 __attribute__((ext_vector_type(16)));
typedef float f32x2 __attribute__((ext_vector_type(2)));
typedef float f32x4 __attribute__((ext_vector_type(4)));
typedef unsigned u32x2 __attribute__((ext_vector_type(2)));
typedef unsigned u32x4 __attribute__((ext_vector_type(4)));
typedef unsigned u32x8 __attribute__((ext_vector_type(8)));
typedef unsigned u32x32 __attribute__((ext_vector_type(32)));
typedef __amdgpu_buffer_rsrc_t rsrc_t;

#define DEV __device__ __forceinline__
#define SC1 16

constexpr int NSEG = 64;
constexpr unsigned MB = 1u << 20;
constexpr unsigned OFF_COMM = 0;
constexpr unsigned OFF_CTRL = 18 * MB;
constexpr unsigned OFF_TS = 19 * MB;
constexpr unsigned OFF_XN = 20 * MB;
constexpr unsigned OFF_BASE = 22 * MB;
constexpr unsigned OFF_P = 26 * MB;
constexpr unsigned OFF_U = 32 * MB;
constexpr unsigned OFF_KF = 32 * MB;
constexpr unsigned OFF_QF = 64 * MB;
constexpr unsigned OFF_VW = 96 * MB;
constexpr unsigned WS_END = 224 * MB;
#define C_EPOCH_ 0x1200000
#define C_MSG_ 0x1200d00
#define C_XF_ 0x1200200
#define C_HM_ 0x1200100
constexpr unsigned C_VOTE = 0x1200c00;
constexpr unsigned C_EPOCH = C_EPOCH_;
constexpr unsigned C_MSG = C_MSG_;
constexpr unsigned C_HM = C_HM_;
constexpr unsigned C_XF = C_XF_;
constexpr unsigned C_CNT = OFF_CTRL + 32768;
constexpr unsigned C_KQF = C_CNT;
constexpr unsigned C_VWF = C_CNT + 4096;
constexpr unsigned C_U2F = C_CNT + 20480;
constexpr unsigned C_PF = C_CNT + 24576;
constexpr unsigned C_BF = C_CNT + 28672;
constexpr unsigned C_ERR = C_CNT + 28672 + 256;
constexpr unsigned C_DBG = OFF_CTRL + 4096;
constexpr unsigned OOR = 0x40000000u;
static_assert(C_EPOCH == OFF_CTRL, "");

constexpr int SPIN_CAP = 1 << 17;
constexpr int LDS_BYTES = 128 * 1024;

#define DLY_QK_M 182
#define DLY_QK_K 50
#define DLY_VO_U 103
#define DLY_D1 10
#define DLY_D2 20
#define DLY_DB 16
#define DLY_VO_M 0

#define DLY_MP 0
#define DLY_PP 80

#define DLY_X 175
#define DLY_Q2 35

#define DLY_PRE 138
#define DLY_PRS 80
#define DLY_PACE 9
#define DLY_FFN 4

#define DLY_E1 3
#define DLY_E2 10
#define XFAR 72
#define LEAD 5

struct Params {
    const void *x, *mem0, *wq, *wk, *wv, *wo, *w1, *w2;
    void* out;
    unsigned char* ws;
    unsigned rolemask;
    unsigned dly[17];
};

DEV rsrc_t mkrs(const void* p, unsigned bytes) { return __builtin_amdgcn_make_buffer_rsrc((void*)p, 0, (int)bytes, 0x00020000); }
DEV u32x4 mksrd(const void* p, unsigned bytes) { return u32x4{(unsigned)(uintptr_t)p, (unsigned)((uintptr_t)p >> 32) & 0xffffu, bytes, 0x00020000u}; }
template <int AUX> DEV u32x4 ld128(rsrc_t r, unsigned v, unsigned s) { return __builtin_amdgcn_raw_buffer_load_b128(r, (int)v, (int)s, AUX); }
template <int AUX> DEV u32x2 ld64(rsrc_t r, unsigned v, unsigned s) { return __builtin_amdgcn_raw_buffer_load_b64(r, (int)v, (int)s, AUX); }
template <int AUX> DEV unsigned ld32(rsrc_t r, unsigned v, unsigned s) { return __builtin_amdgcn_raw_buffer_load_b32(r, (int)v, (int)s, AUX); }
template <int AUX> DEV void st128(u32x4 d, rsrc_t r, unsigned v, unsigned s) { __builtin_amdgcn_raw_buffer_store_b128(d, r, (int)v, (int)s, AUX); }
template <int AUX> DEV void st64(u32x2 d, rsrc_t r, unsigned v, unsigned s) { __builtin_amdgcn_raw_buffer_store_b64(d, r, (int)v, (int)s, AUX); }
template <int AUX> DEV void st32(unsigned d, rsrc_t r, unsigned v, unsigned s) { __builtin_amdgcn_raw_buffer_store_b32(d, r, (int)v, (int)s, AUX); }

DEV unsigned pk(float lo, float hi) { f32x2 v = {lo, hi}; return __builtin_bit_cast(unsigned, __builtin_convertvector(v, bf16x2)); }
DEV unsigned plo(unsigned a, unsigned b) { return __builtin_amdgcn_perm(b, a, 0x05040100u); }
DEV unsigned phi(unsigned a, unsigned b) { return __builtin_amdgcn_perm(b, a, 0x07060302u); }
DEV float asf(unsigned u) { return __builtin_bit_cast(float, u); }
DEV unsigned asu(float f) { return __builtin_bit_cast(unsigned, f); }
DEV f32x4 mfma(u32x4 a, u32x4 b, f32x4 c) {
    return __builtin_amdgcn_mfma_f32_16x16x32_bf16(__builtin_bit_cast(bf16x8, a), __builtin_bit_cast(bf16x8, b), c, 0, 0, 0);
}
DEV f32x4 smf(u32x4 a, u32x8 b, f32x4 c, int idx) {
    return __builtin_amdgcn_smfmac_f32_16x16x64_bf16(__builtin_bit_cast(bf16x8, a), __builtin_bit_cast(bf16x16, b), c, idx, 0, 0);
}
DEV u32x8 cat(u32x4 a, u32x4 b) { return __builtin_shufflevector(a, b, 0, 1, 2, 3, 4, 5, 6, 7); }
DEV float fold32(float a, float b) { auto r = __builtin_amdgcn_permlane32_swap(asu(a), asu(b), false, false); return asf(r[0]) + asf(r[1]); }
DEV float max32(float a) { auto r = __builtin_amdgcn_permlane32_swap(asu(a), asu(a), false, false); return fmaxf(asf(r[0]), asf(r[1])); }
DEV float max16(float a) { auto r = __builtin_amdgcn_permlane16_swap(asu(a), asu(a), false, false); return fmaxf(asf(r[0]), asf(r[1])); }
DEV float add16(float a) { auto r = __builtin_amdgcn_permlane16_swap(asu(a), asu(a), false, false); return asf(r[0]) + asf(r[1]); }
template <int CTRL> DEV float dppf(float a) { return asf(__builtin_amdgcn_update_dpp(0u, asu(a), CTRL, 0xf, 0xf, true)); }
DEV float rowmax(float a) { a = fmaxf(a, dppf<0x128>(a)); a = fmaxf(a, dppf<0x124>(a)); a = fmaxf(a, dppf<0x122>(a)); return fmaxf(a, dppf<0x121>(a)); }
DEV float rowsum(float a) { a += dppf<0x128>(a); a += dppf<0x124>(a); a += dppf<0x122>(a); return a + dppf<0x121>(a); }
DEV unsigned umax4(u32x4 a) { return max(max(a.x, a.y), max(a.z, a.w)); }
DEV unsigned pofs(unsigned o) { return 16384 * (6 + (o >> 13)) + (o & 8191); }
DEV unsigned nosent(unsigned a) { return min(a, 0xFFFFFFFEu); }

#define PIN_A(x) asm volatile("" : "+a"(x))
#define SCHED() __builtin_amdgcn_sched_barrier(0)
#define RFL(x) __builtin_amdgcn_readfirstlane(x)
#define WAIT_VM0() asm volatile("s_waitcnt vmcnt(0)" ::: "memory")
#define BAR() do { asm volatile("s_waitcnt lgkmcnt(0)" ::: "memory"); __builtin_amdgcn_s_barrier(); asm volatile("" ::: "memory"); } while (0)

DEV void delay(int n) {
    int t;
    asm volatile("s_mov_b32 %0, %1\n\t.p2align 6\n"
                 "1:\n\ts_sub_u32 %0, %0, 1\n\ts_cmp_gt_i32 %0, 0\n\ts_cbranch_scc1 1b" : "=&s"(t) : "s"(n) : "scc", "memory");
}

template <int NW> DEV unsigned wait_flags(rsrc_t rws, unsigned off, unsigned need, int& budget, int lane) {
    unsigned v;
    const unsigned a = off + (lane & (NW - 1)) * 128;
#pragma nounroll
    for (;;) {
        v = ld32<SC1>(rws, a, 0);
        if (__all(v > need) || --budget < 0) break;
        __builtin_amdgcn_s_sleep(4);
    }
    asm volatile("" ::: "memory");
    unsigned m = __builtin_amdgcn_readlane(v, 0);
#pragma unroll
    for (int i = 1; i < NW; ++i) m = min(m, (unsigned)__builtin_amdgcn_readlane(v, i));
    return m;
}

DEV void glds16(const unsigned char* g, char* l) {
    __builtin_amdgcn_global_load_lds((const __attribute__((address_space(1))) void*)g, (__attribute__((address_space(3))) void*)l, 16, 0, 0);
}

#define STAMP(slot)

constexpr float SC_LOG2 = 0.08838834764831845f * 1.4426950408889634f;

#define SMF "v_smfmac_f32_16x16x64_bf16 "
#define DPPM " row_mask:0xf bank_mask:0xf\n"
#define Z4(a, b, c, d) "v_mov_b32 v" #a ", 0\nv_mov_b32 v" #b ", 0\nv_mov_b32 v" #c ", 0\nv_mov_b32 v" #d ", 0\n"
#define DELAY(n, L) "s_mov_b32 s60, %[" n "]\n.p2align 6\n" L "%=:\ns_sub_u32 s60, s60, 1\ns_cmp_gt_i32 s60, 0\ns_cbranch_scc1 " L "%=\n"
#define PT(k)
#define PFLUSH
#define PDUMP(cu)
#define CL8(p) "v" #p "0", "v" #p "1", "v" #p "2", "v" #p "3", "v" #p "4", "v" #p "5", "v" #p "6", "v" #p "7", "v" #p "8", "v" #p "9"
#define AC8(p) "a" #p "0", "a" #p "1", "a" #p "2", "a" #p "3", "a" #p "4", "a" #p "5", "a" #p "6", "a" #p "7", "a" #p "8", "a" #p "9"
#define ACL AC8(), AC8(1), AC8(2), AC8(3), AC8(4), AC8(5), AC8(6), AC8(7), AC8(8), AC8(9), AC8(10), AC8(11), AC8(12), AC8(13), AC8(14), AC8(15), AC8(16), \
            AC8(17), AC8(18), AC8(19), AC8(20), AC8(21), AC8(22), AC8(23), AC8(24), "a250", "a251", "a252", "a253", "a254", "a255"
#define CLM "v56", "v57", "v58", "v59", CL8(6), CL8(7), CL8(8), "v90", "v91", "v92", "v93", "v94", "v95"
#define SCL "s60", "s61", "s62", "s63", "s64", "s65", "s66", "s67", "s68", "s69", "s70", "s71", "s72", "s73", "s74", "s75", "s76", "s77", "s78", "s79", \
            "s80", "s81", "s82", "s83", "s84", "s85", "s86", "s87", "s88", "s89", "s90", "s91", "s92", "s93", "s94", "s95"

#define Q4(A, b0, b1, b2, b3, X, Y) SMF "v[128:131], " A ", a[" b0 "], %[idx]\n" X SMF "v[132:135], " A ", a[" b1 "], %[idx]\n" \
                                    SMF "v[136:139], " A ", a[" b2 "], %[idx]\n" Y SMF "v[140:143], " A ", a[" b3 "], %[idx]\n"
#define QZERO Z4(128, 129, 130, 131) Z4(132, 133, 134, 135) Z4(136, 137, 138, 139) Z4(140, 141, 142, 143)
#define SZERO Z4(232, 233, 234, 235) Z4(236, 237, 238, 239) Z4(240, 241, 242, 243)
#define QK_WISSUE \
    "s_add_u32 s61, %[w], wl_t/2\ns_and_b32 s61, s61, 3\ns_lshl_b32 s61, s61, 20\n" \
    ".set wl_i, 0\n.rept 16\n" \
    "s_add_u32 s62, s61, ((wl_t&1)*4 + 2*(wl_i>>3) + 64*((wl_i>>1)&1) + 16*((wl_i>>2)&1) + 8*(wl_i&1))*8192\n" \
    "buffer_load_dwordx2 v[128+(wl_t&3)*32+wl_i*2:129+(wl_t&3)*32+wl_i*2], %[wvo], %[rw], s62 offen\n" \
    ".set wl_i, wl_i+1\n.endr\n.set wl_t, wl_t+1\n"
#define QK_WLOAD \
    "s_mov_b32 s63, 0x05040100\ns_mov_b32 s64, 0x07060302\n" \
    ".set wl_t, 0\n.rept 3\n" QK_WISSUE ".endr\n" \
    ".set wl_p, 0\n.rept 8\n" \
    ".if wl_p < 6\ns_waitcnt vmcnt(32)\n.elseif wl_p == 6\ns_waitcnt vmcnt(16)\n.else\ns_waitcnt vmcnt(0)\n.endif\n" \
    ".if wl_t < 8\n" QK_WISSUE ".endif\n" \
    ".set wl_i, 0\n.rept 8\n" \
    ".set wl_b, 128+(wl_p&3)*32+wl_i*4\n" \
    "v_perm_b32 v120, v[wl_b+2], v[wl_b], s63\nv_perm_b32 v121, v[wl_b+2], v[wl_b], s64\n" \
    "v_perm_b32 v122, v[wl_b+3], v[wl_b+1], s63\nv_perm_b32 v123, v[wl_b+3], v[wl_b+1], s64\n" \
    "v_accvgpr_write_b32 a[wl_p*32+wl_i], v120\nv_accvgpr_write_b32 a[wl_p*32+8+wl_i], v121\n" \
    "v_accvgpr_write_b32 a[wl_p*32+16+wl_i], v122\nv_accvgpr_write_b32 a[wl_p*32+24+wl_i], v123\n" \
    ".set wl_i, wl_i+1\n.endr\n.set wl_p, wl_p+1\n.endr\n"
#define STR2(x) #x
#define STR(x) STR2(x)
#define CLRWAIT(msg) \
    "v_mbcnt_lo_u32_b32 v248, -1, 0\nv_mbcnt_hi_u32_b32 v248, -1, v248\n" \
    "s_mov_b32 s60, " STR(C_EPOCH_) "\ns_mov_b32 s62, " STR(C_XF_) "\n" \
    ".if " #msg "\ns_mov_b32 s61, " STR(C_MSG_) "\n.else\ns_mov_b32 s61, " STR(C_HM_) "\n.endif\n" \
    "v_lshlrev_b32 v248, 3, v248\nv_and_b32 v249, 0xff*" #msg ", v248\n" \
    "buffer_load_dwordx2 v[250:251], off, %[srd], s60 sc1\n" \
    "Lclr%=:\n" \
    "buffer_load_dwordx2 v[254:255], v248, %[srd], s62 offen sc1\n" \
    "buffer_load_dwordx2 v[252:253], v249, %[srd], s61 offen sc1\n" \
    "s_waitcnt vmcnt(0)\n" \
    "v_readfirstlane_b32 s64, v250\nv_readfirstlane_b32 s65, v251\n" \
    "s_add_u32 s64, s64, 0x10000\ns_addc_u32 s65, s65, 0\ns_or_b32 s64, s64, 0xffff\n" \
    "v_cmp_ne_u32 vcc, s64, v254\n" \
    "v_cmp_ne_u32 s[66:67], s65, v255\n" \
    "s_or_b64 vcc, vcc, s[66:67]\n" \
    "v_or_b32 v247, 0xffff, v252\nv_cmp_ne_u32 s[66:67], s64, v247\ns_or_b64 vcc, vcc, s[66:67]\nv_cmp_ne_u32 s[66:67], s65, v253\ns_or_b64 vcc, vcc, s[66:67]\n" \
    "s_cmp_eq_u64 vcc, 0\n" \
    "s_cbranch_scc1 Lclrd%=\n" \
    "s_sub_u32 %[bud], %[bud], 1\n" \
    "s_sleep 4\n" \
    "s_cmp_lt_i32 %[bud], 0\n" \
    "s_cbranch_scc0 Lclr%=\n" \
    "Lclrd%=:\n" \
    "v_readfirstlane_b32 s61, v252\n" \
    "s_bitcmp1_b32 s61, 10\ns_cselect_b32 s63, 0x2000, 0\n" \
    "s_bitcmp1_b32 s61, 9\ns_cselect_b32 s62, 0x2000, 0\n"
#define BIGSO(d, st, sh, rs) "s_lshl_b32 s60, " st ", 13-" #rs "\ns_and_b32 s60, s60, 0x2000\ns_xor_b32 s60, s60, s72\ns_lshl_b32 " d ", " st ", " #sh "\ns_or_b32 " d ", " d ", s60\n"
#define CLRSO "s_mov_b32 %[hm], s61\nv_add_u32 %[tso], s62, %[tso]\n"
#define SETSO(d, st) "s_lshr_b32 s60, " st ", 3\ns_bitcmp1_b32 %[hm], s60\ns_cselect_b32 s60, 0x2000, 0\ns_lshl_b32 " d ", " st ", 18\ns_or_b32 " d ", " d ", s60\n"
#define WAITT(off, L) \
    "s_add_u32 s65, s79, " off "\n" \
    L "a%=:\n" \
    "s_memrealtime s[76:77]\n" \
    "s_waitcnt lgkmcnt(0)\n" \
    "s_sub_i32 s78, s65, s76\n" \
    "s_cmp_gt_i32 s78, 16\n" \
    "s_cbranch_scc1 " L "a%=\n" \
    "s_mul_i32 s60, s78, 3\n" \
    "s_ashr_i32 s60, s60, 2\n" \
    ".p2align 6\n" L "b%=:\ns_sub_u32 s60, s60, 1\ns_cmp_gt_i32 s60, 0\ns_cbranch_scc1 " L "b%=\n"
#define GRID "Lpoll%=:\n" WAITT("0", "Lw") "s_cmp_lt_i32 s78, -10\ns_cselect_b32 s79, s76, s79\n" PT(0)
#define TSLOAD "s_lshl_b32 s60, %[s], 14\nbuffer_load_dword v251, %[tso], %[srd], s60 offen sc1\n"
#define TSMAX(n) \
    "s_waitcnt vmcnt(" #n ")\n" \
    "v_cmp_eq_u32 vcc, -1, v251\n" \
    "v_subrev_u32 v251, s79, v251\n" \
    "s_nop 1\nv_max_i32_dpp v251, v251, v251 row_ror:8" DPPM \
    "s_nop 1\nv_max_i32_dpp v251, v251, v251 row_ror:4" DPPM \
    "s_nop 1\nv_max_i32_dpp v251, v251, v251 row_ror:2" DPPM \
    "s_nop 1\nv_max_i32_dpp v251, v251, v251 row_ror:1" DPPM \
    "s_nop 0\nv_readlane_b32 s76, v251, 0\nv_readlane_b32 s77, v251, 16\n" \
    "s_max_i32 s76, s76, s77\ns_min_i32 s76, s76, 0x400\n"
#define TSFOLLOW(n) \
    TSMAX(n) \
    "s_add_u32 s76, s76, %[gn]\n" \
    "s_cmp_eq_u64 vcc, 0\n" \
    "s_cselect_b32 s76, s76, %[gd]\n" \
    "s_add_u32 s79, s79, s76\n"
#define MLD(b, so) \
    "buffer_load_dwordx4 v[" #b ":" #b "+3], %[mo], %[srd], " so " offen sc1\n" \
    "buffer_load_dwordx4 v[" #b "+4:" #b "+7], %[mo], %[srd], " so " offen offset:1024 sc1\n"
#define MISSUE(b) \
    MLD(b, "%[so]") "s_mov_b32 s75, 0\ns_add_u32 s61, %[so], 0x4000\ns_add_u32 s62, %[so], 0x8000\n" \
    DELAY("i1", "Lda") MLD(56, "s61") DELAY("i2", "Ldb") MLD(64, "s62") "Lchk1%=:\n"
#define MV8(d, s) ".set wl_i, 0\n.rept 8\nv_mov_b32 v[" #d "+wl_i], v[" #s "+wl_i]\n.set wl_i, wl_i+1\n.endr\n"
#define MUSE(b, k, n, r) \
    ".if " #k " <= 3\ns_add_u32 s61, %[so], 0x8000+" #k "*0x4000\n" MLD(64+k*8, "s61") ".endif\n" \
    "s_waitcnt vmcnt(" #n ")\n" \
    "v_max3_u32 v244, v[" #r "], v[" #r "+1], v[" #r "+2]\nv_max3_u32 v245, v[" #r "+3], v[" #r "+4], v[" #r "+5]\n" \
    "v_max3_u32 v244, v244, v[" #r "+6], v[" #r "+7]\nv_max_u32 v244, v244, v245\n" \
    "v_cmp_eq_u32 vcc, -1, v244\n" \
    "s_cbranch_vccnz Lre" #k "%=\n" \
    MV8(b, r) "s_mov_b32 s75, " #k "\ns_branch Lchk1%=\n" \
    "Lre" #k "%=:\n"
#define MRETRY(b) \
    "Lretry%=:\n" \
    "s_nop 3\n" Z4(128, 129, 130, 131) Z4(132, 133, 134, 135) \
    MUSE(b, 1, 4, 56) MUSE(b, 2, 4, 64) MUSE(b, 3, 4, 72) MUSE(b, 4, 2, 80) MUSE(b, 5, 0, 88) \
    "s_sub_u32 %[bud], %[bud], 1\n" \
    "s_add_u32 s79, s79, %[gq]\n" \
    "s_cmp_lt_i32 %[bud], 0\n" \
    "s_cbranch_scc0 Lpoll%=\n" \
    "s_branch Lgo%=\n"
#define MREAD(r) \
    "s_waitcnt lgkmcnt(0)\n" \
    "s_barrier\n" \
    "ds_read_b128 v[" #r ":" #r "+3], %[ap1]\nds_read_b128 v[" #r "+4:" #r "+7], %[ap1] offset:1024\n" \
    "ds_read_b128 v[" #r "+8:" #r "+11], %[ap2]\nds_read_b128 v[" #r "+12:" #r "+15], %[ap2] offset:1024\n" \
    "ds_read_b128 v[" #r "+16:" #r "+19], %[ap3]\nds_read_b128 v[" #r "+20:" #r "+23], %[ap3] offset:1024\n"
#define KRD(i) "ds_read_b128 v[200+" #i "*4:203+" #i "*4], %[kr] offset:" #i "*1024\n"
#define QRD(i) "ds_read_b128 v[176+" #i "*4:179+" #i "*4], %[qr] offset:" #i "*1024\n"
#define KDMA(i) "buffer_load_dwordx4 %[kxo], %[srd], s73 offen offset:" #i "*1024 sc1 lds\n"
#define QK_HEAD(X5, X6, n6) \
    GRID MISSUE(144) \
    "s_waitcnt vmcnt(5)\n" \
    SMF "v[128:131], v[144:147], a[0:7], %[idx]\n" \
    "ds_write_b128 %[mo], v[144:147]\n" \
    "v_max3_u32 v244, v144, v145, v146\n" \
    SMF "v[132:135], v[144:147], a[8:15], %[idx]\n" \
    "s_waitcnt vmcnt(4)\n" \
    "ds_write_b128 %[mo], v[148:151] offset:1024\n" \
    "v_max3_u32 v245, v147, v148, v149\n" \
    "v_max3_u32 v244, v244, v150, v151\n" \
    "v_max_u32 v244, v244, v245\n" \
    "v_cmp_eq_u32 vcc, -1, v244\n" \
    "s_cbranch_vccnz Lretry%=\n" \
    "Lgo%=:\n" PT(1) \
    MREAD(152) \
    SMF "v[136:139], v[144:147], a[16:23], %[idx]\n" SMF "v[140:143], v[144:147], a[24:31], %[idx]\n" \
    Q4("v[148:151]", "32:39", "40:47", "48:55", "56:63", "", "") \
    "s_waitcnt lgkmcnt(5)\n" Q4("v[152:155]", "64:71", "72:79", "80:87", "88:95", KRD(0) KRD(1), "") \
    "s_waitcnt lgkmcnt(6)\n" Q4("v[156:159]", "96:103", "104:111", "112:119", "120:127", KRD(2) KRD(3), "") \
    "s_waitcnt lgkmcnt(7)\n" Q4("v[160:163]", "128:135", "136:143", "144:151", "152:159", KRD(4) KRD(5), "") \
    "s_waitcnt lgkmcnt(8)\n" Q4("v[164:167]", "160:167", "168:175", "176:183", "184:191", KRD(6) KRD(7), "") \
    "s_waitcnt lgkmcnt(9)\n" Q4("v[168:171]", "192:199", "200:207", "208:215", "216:223", X5, "") \
    "s_waitcnt lgkmcnt(" #n6 ")\n" Q4("v[172:175]", "224:231", "232:239", "240:247", "248:255", X6, "") \
    "s_nop 1\n" \
    "v_permlane32_swap_b32 v128, v129\nv_permlane32_swap_b32 v130, v131\n" \
    "v_add_f32 v128, v128, v129\nv_add_f32 v130, v130, v131\n" \
    "v_permlane32_swap_b32 v132, v133\nv_permlane32_swap_b32 v134, v135\n" \
    "v_add_f32 v132, v132, v133\nv_add_f32 v134, v134, v135\n" \
    "v_cvt_pk_bf16_f32 v244, v128, v132\nv_cvt_pk_bf16_f32 v246, v130, v134\n" \
    "v_permlane32_swap_b32 v136, v137\nv_permlane32_swap_b32 v138, v139\n" \
    "v_permlane32_swap_b32 v140, v141\nv_permlane32_swap_b32 v142, v143\n" \
    "v_add_f32 v136, v136, v137\nv_add_f32 v140, v140, v141\n" \
    "v_cvt_pk_bf16_f32 v245, v136, v140\n"
#define QK_MID \
    "v_add_f32 v138, v138, v139\nv_add_f32 v142, v142, v143\n" \
    "v_cvt_pk_bf16_f32 v247, v138, v142\n"
#define QK_TAIL(n) \
    PFLUSH TSFOLLOW(n) PT(4) \
    "s_add_u32 %[s], %[s], 1\n" \
    SETSO("%[so]", "%[s]") \
    "s_branch Lpoll%=\n" \
    MRETRY(144) \
    "Lend%=:\ns_waitcnt vmcnt(0)\n"

#define KQFLOAD \
    "buffer_load_dwordx4 v[244:247], off, %[srd], %[fq] sc1\n" \
    "s_waitcnt vmcnt(0)\n" \
    "v_min3_u32 v244, v244, v245, v246\n" \
    "v_min_u32 v244, v244, v247\n" \
    "s_nop 0\n" \
    "v_readfirstlane_b32 %[seen], v244\n"
#define KQFPRE "s_add_u32 s74, %[s], " STR(LEAD) "\ns_min_u32 s74, s74, 64\ns_cmp_lt_u32 %[seen], s74\ns_cselect_b32 s74, %[fq], 0x40000000\nbuffer_load_dwordx4 v[252:255], off, %[srd], s74 sc1\n"
#define QK_NEXT \
    TSLOAD KQFPRE \
    "Lnext%=:\n" \
    "s_add_u32 s63, %[s], 1\n" \
    "s_cmp_gt_u32 %[seen], s63\n" \
    "s_cbranch_scc1 Lgot%=\n" \
    "Lflag%=:\n" \
    KQFLOAD \
    "s_cmp_gt_u32 %[seen], s63\n" \
    "s_cbranch_scc1 Lgot%=\n" \
    "s_sub_u32 %[bud], %[bud], 1\n" \
    "s_sleep 2\n" \
    "s_cmp_lt_i32 %[bud], 0\n" \
    "s_cbranch_scc0 Lflag%=\n" \
    "Lgot%=:\n" \
    BIGSO("s73", "s63", 19, 2) \
    KDMA(0) KDMA(1) KDMA(2) KDMA(3) \
    "s_waitcnt vmcnt(4)\n" \
    "v_min3_u32 v252, v252, v253, v254\n" \
    "v_min_u32 v252, v252, v255\n" \
    "s_nop 0\n" \
    "v_readfirstlane_b32 s74, v252\n" \
    "s_max_u32 %[seen], %[seen], s74\n"
#define QK_INIT \
    "s_xor_b32 s72, s63, 0x2000\n" \
    "s_mov_b32 %[s], -1\ns_mov_b32 s79, %[t0]\ns_mov_b32 s75, 0\n" \
    "s_lshl_b32 s60, %[w], 12\ns_add_u32 m0, s60, 0x8000\n" \
    "v_mov_b32 v251, -1\nv_mov_b32 v252, 0\n" \
    "s_branch Lnext%=\n"
#define MF "v_mfma_f32_16x16x32_bf16 "

DEV u32x2 next_tag(u32x2 e) {
    asm volatile("" : "+v"(e));
    const unsigned long long t = (((unsigned long long)(unsigned)RFL(e.y) << 32) | (unsigned)RFL(e.x)) + 0x10000;
    return u32x2{(unsigned)t & 0xffff0000u, (unsigned)(t >> 32)};
}
DEV unsigned wait_tags(rsrc_t rws, unsigned off, u32x2 tag, int& budget) {
    u32x2 v;
#pragma nounroll
    for (;;) {
        v = ld64<SC1>(rws, off, 0);
        if (__all((v.x & 0xffff0000u) == tag.x && v.y == tag.y) || --budget < 0) break;
        __builtin_amdgcn_s_sleep(4);
    }
    asm volatile("" ::: "memory");
    return v.x & 0xffffu;
}
constexpr unsigned vmask(int r, int w) { unsigned m = 0; for (int l = 0; l < 32; ++l) if ((4 * l + w) % 10 == r) m |= 1u << l; return m; }
DEV unsigned close_halves(rsrc_t rws, u32x2 tag, int lane, int& budget) {
    const unsigned v = wait_tags(rws, C_VOTE + (lane & 31) * 8, tag, budget);
    const unsigned b0 = (unsigned)__ballot(v & 1), b1 = (unsigned)__ballot(v & 2), b2 = (unsigned)__ballot(v & 4), b3 = (unsigned)__ballot(v & 8);
    unsigned hm = 0;
#pragma unroll
    for (int r = 0; r < 10; ++r)
        hm |= (__builtin_popcount(b0 & vmask(r, 0)) + __builtin_popcount(b1 & vmask(r, 1)) + __builtin_popcount(b2 & vmask(r, 2)) + __builtin_popcount(b3 & vmask(r, 3)) > 6 ? 1u : 0u) << r;
    return hm | (__builtin_popcount(hm ^ 0x2aau) > 5 ? 1024u : 0u);
}
DEV void reset_piece(rsrc_t rws, int b, int id, int tid, unsigned hm) {
    const unsigned o = id < 27 ? OFF_COMM + b * 262144 + ((hm >> (b >> 3)) & 1) * 8192 + (id >> 1) * 16384 + (id & 1) * 4096 : OFF_TS + b * 16384 + ((hm >> 9) & 1) * 8192;
    st128<SC1>(u32x4{~0u, ~0u, ~0u, ~0u}, rws, (id > 27 || b > 63 || (b == 0 && id < 12)) ? OOR : o + tid * 16, 0);
}

DEV void role_qk(const Params& P, rsrc_t rws, u32x4 srd, char* lds, int h, int w, int lane) {
    const int g = lane >> 4, c = lane & 15;
    const u32x4 rw = mksrd(w < 2 ? P.wq : P.wk, 4u << 20);
    const unsigned wvo = ((g >> 1) + 32 * (g & 1)) * 8192 + h * 256 + (64 * (w & 1) + 4 * c) * 2;
    int budget = SPIN_CAP, s;
    const int idx = (c < 8) ? 0x4444 : 0xEEEE;
    const int rho0 = 4 * (g & 1) + (g >> 1);
    const unsigned mo = w * 2048 + lane * 16;
    const unsigned ap1 = ((2 * w + 2) & 7) * 1024 + lane * 16, ap2 = ((2 * w + 4) & 7) * 1024 + lane * 16, ap3 = ((2 * w + 6) & 7) * 1024 + lane * 16;
    unsigned so, hm;
    unsigned pf = 16384 + w * 2048;
    const unsigned t0 = (unsigned)__builtin_amdgcn_s_memrealtime() - P.dly[0];
    unsigned tso = lane < 32 ? OFF_TS + lane * 128 : OOR;
    const unsigned i1 = P.dly[3] * 3 / 4, i2 = (P.dly[13] - P.dly[3]) * 3 / 4;
    const unsigned gn = P.dly[0] - P.dly[2] + P.dly[5];
    const unsigned kxo = (w < 2 ? OFF_KF : OFF_QF) + h * 16384 + (w & 1) * 4096 + lane * 16;
    const unsigned kr = 32768 + lane * 16;
    const unsigned fq = C_KQF + h * 128;
    unsigned seen = 0;
    pf -= 32;
    const unsigned kra0 = 10240 + g * 128 + (((c & 7) + 2 * (g >> 1)) & 7) * 16, kra1 = 10240 + 512 + g * 128 + (((c & 7) + 1 + 2 * (g >> 1)) & 7) * 16;
    if (w < 2) {
        const int ga = 2 * ((c >> 1) & 1) + (c >> 3), i = 2 * ((c >> 2) & 1) + (c & 1);
        const unsigned wa0 = 8192 + w * 1024 + ga * 256 + ((rho0 + 2 * (ga & 1)) & 7) * 16 + i * 4;
        const unsigned wa1 = 8192 + w * 1024 + ga * 256 + ((rho0 + 2 + 2 * (ga & 1)) & 7) * 16 + i * 4;
        const int rq = 4 * w + 2 * ((c >> 2) & 1) + (c & 1);
        const unsigned qra = 8192 + g * 256 + (c >> 3) * 128 + ((rq + 2 * (g & 1)) & 7) * 16;
        const int r = 4 * w + 2 * (g & 1) + (g >> 1);
        unsigned pox = h * 640 + (c >> 3) * 256 + (c & 1) * 128 + r * 16 + ((c >> 1) & 3) * 4;
        unsigned pom = h * 640 + 512 + r * 16 + ((c & 7) >> 1) * 4;
        const unsigned pbx = pofs(pox + 20480), pbm = pofs(pom + 20480), pcx = pofs(pox + 40960), pcm = pofs(pom + 40960);
        pox = pofs(pox); pom = pofs(pom);
        asm volatile(
            QK_WLOAD CLRWAIT(1) CLRSO
            QK_INIT
            QK_HEAD("", "", 8)
            "ds_write2_b32 %[wa0], v244, v245 offset1:32\n"
            QK_MID
            "ds_write2_b32 %[wa1], v246, v247 offset1:32\n"
            "s_cmp_eq_u32 %[s], 63\n"
            "s_waitcnt lgkmcnt(0)\n"
            "s_barrier\n" PT(2)
            "s_cbranch_scc1 Lend%=\n"
            "ds_read_b128 v[176:179], %[qra]\nds_read_b128 v[180:183], %[qra] offset:1024\n"
            "ds_read_b128 v[184:187], %[kra0]\nds_read_b128 v[188:191], %[kra1]\n"
            "ds_read_b128 v[192:195], %[kra0] offset:1024\nds_read_b128 v[196:199], %[kra1] offset:1024\n"
            "s_waitcnt lgkmcnt(5)\n"
            SMF "v[232:235], v[176:179], v[200:207], %[idx]\n"
            SMF "v[236:239], v[176:179], v[216:223], %[idx]\n"
            "s_waitcnt lgkmcnt(4)\n"
            SMF "v[232:235], v[180:183], v[208:215], %[idx]\n"
            SMF "v[236:239], v[180:183], v[224:231], %[idx]\n"
            "s_waitcnt lgkmcnt(2)\n"
            SMF "v[240:243], v[176:179], v[184:191], %[idx]\n"
            "s_waitcnt lgkmcnt(0)\n"
            SMF "v[240:243], v[180:183], v[192:199], %[idx]\n"
            "v_permlane32_swap_b32 v232, v233\n"
            "v_add_f32 v232, v232, v233\n"
            "v_permlane32_swap_b32 v236, v237\n"
            "v_add_f32 v233, v236, v237\n"
            "v_max_f32 v244, v232, v233\n"
            "s_nop 1\n"
            "v_max_f32_dpp v244, v244, v244 row_ror:8" DPPM
            "v_permlane32_swap_b32 v240, v241\n"
            "v_add_f32 v240, v240, v241\n"
            "v_max_f32_dpp v244, v244, v244 row_ror:4" DPPM
            "s_nop 0\n"
            "v_max_f32_dpp v245, v240, v240 row_ror:4" DPPM
            "v_max_f32_dpp v244, v244, v244 row_ror:2" DPPM
            "s_nop 0\n"
            "v_max_f32_dpp v245, v245, v245 row_ror:2" DPPM
            "v_max_f32_dpp v244, v244, v244 row_ror:1" DPPM
            "s_nop 0\n"
            "v_max_f32_dpp v245, v245, v245 row_ror:1" DPPM
            "v_max_f32 v244, v244, v245\n"
            "v_mul_f32 v244, %[sc], v244\n"
            "v_fma_f32 v232, v232, %[sc], -v244\n"
            "v_fma_f32 v233, v233, %[sc], -v244\n"
            "v_exp_f32 v232, v232\n"
            "v_fma_f32 v240, v240, %[sc], -v244\n"
            "v_exp_f32 v233, v233\n"
            "v_exp_f32 v240, v240\n"
            "v_add_f32 v246, v232, v233\n"
            "v_fmac_f32 v246, 0.5, v240\n"
            "s_nop 0\n"
            "v_mov_b32_dpp v247, v240 quad_perm:[1,0,3,2]" DPPM
            "v_add_f32_dpp v246, v246, v246 row_ror:8" DPPM
            "v_cndmask_b32 v248, v240, v247, %[odd]\n"
            "v_cndmask_b32 v249, v247, v240, %[odd]\n"
            "v_add_f32_dpp v246, v246, v246 row_ror:4" DPPM
            "s_nop 1\n"
            "v_add_f32_dpp v246, v246, v246 row_ror:2" DPPM
            "s_nop 1\n"
            "v_add_f32_dpp v246, v246, v246 row_ror:1" DPPM
            "v_rcp_f32 v246, v246\n"
            "s_nop 0\n"
            "v_pk_mul_f32 v[232:233], v[232:233], v[246:247] op_sel_hi:[1,0] clamp\n"
            "v_cvt_pk_bf16_f32 v232, v232, v233\n"
            "buffer_store_dword v232, %[pox], %[srd], %[so] offen sc1\n"
            "v_pk_mul_f32 v[248:249], v[248:249], v[246:247] op_sel_hi:[1,0] clamp\n"
            "v_cvt_pk_bf16_f32 v248, v248, v249\n"
            "buffer_store_dword v248, %[pom], %[srd], %[so] offen sc1\n" PT(3)
            "buffer_store_dword v232, %[pbx], %[srd], %[so] offen sc1\n"
            "buffer_store_dword v248, %[pbm], %[srd], %[so] offen sc1\n"
            "buffer_store_dword v232, %[pcx], %[srd], %[so] offen sc1\n"
            "buffer_store_dword v248, %[pcm], %[srd], %[so] offen sc1\n"
            QK_NEXT
            QZERO SZERO
            QK_TAIL(4)
            : [s] "=&s"(s), [so] "=&s"(so), [hm] "=&s"(hm), [bud] "+s"(budget), [seen] "+s"(seen), [pf] "+v"(pf), [tso] "+v"(tso)
            : [rw] "s"(rw), [wvo] "v"(wvo), [srd] "s"(srd), [mo] "v"(mo), [idx] "v"(idx), [ap1] "v"(ap1), [ap2] "v"(ap2), [ap3] "v"(ap3), [wa0] "v"(wa0), [wa1] "v"(wa1),
              [qra] "v"(qra), [kra0] "v"(kra0), [kra1] "v"(kra1), [pox] "v"(pox), [pom] "v"(pom), [pbx] "v"(pbx), [pbm] "v"(pbm), [pcx] "v"(pcx), [pcm] "v"(pcm), [kr] "v"(kr), [kxo] "v"(kxo), [i1] "s"(i1), [i2] "s"(i2),
              [gd] "s"(P.dly[0]), [gq] "s"(P.dly[8]), [gn] "s"(gn), [t0] "s"(t0), [fq] "s"(fq), [sc] "s"(SC_LOG2), [odd] "s"(0xAAAAAAAAAAAAAAAAull), [w] "s"(w)
            : ACL, CLM, CL8(12), CL8(13), CL8(14), CL8(15), CL8(16), CL8(17), CL8(18), CL8(19), CL8(20), CL8(21), CL8(22), CL8(23), CL8(24),
              "v250", "v251", "v252", "v253", "v254", "v255", SCL, "vcc", "scc", "memory");
    } else {
        const int rot = ((c >> 1) & 1) + 2 * (c >> 3);
        const unsigned wa0 = 10240 + (w - 2) * 1024 + ((c >> 1) & 1) * 512 + (c >> 2) * 128 + ((rho0 + rot) & 7) * 16 + (c & 1) * 8;
        const unsigned wa1 = 10240 + (w - 2) * 1024 + ((c >> 1) & 1) * 512 + (c >> 2) * 128 + ((rho0 + 2 + rot) & 7) * 16 + (c & 1) * 8;
        const int tt = w - 2, tq = 16 * tt + c - 4 * g;
        u32x8 cm;
#pragma unroll
        for (int r = 0; r < 4; ++r) { cm[r] = r <= tq ? 0u : asu(-1.0e30f); cm[4 + r] = 16 + r <= tq ? 0u : asu(-1.0e30f); }
        const unsigned qr = 32768 + 8192 + tt * 4096 + lane * 16;
        const unsigned pxo = OFF_P + tt * 40960 + h * 1024 + lane * 16;
        const unsigned pmo = OFF_P + tt * 40960 + (32 + (h >> 2)) * 1024 + ((h & 3) * 16 + c) * 16 + (g & 1) * 8;
        const unsigned fk = (w == 2 && lane == 0) ? C_PF + h * 128 : OOR;
        asm volatile(
            QK_WLOAD CLRWAIT(1) CLRSO
            QK_INIT
            QK_HEAD(QRD(0) QRD(1), QRD(2) QRD(3), 10)
            "ds_write_b64 %[wa0], v[244:245]\n"
            QK_MID
            "ds_write_b64 %[wa1], v[246:247]\n"
            "s_waitcnt lgkmcnt(0)\n"
            "s_barrier\n" PT(2)
            MF "v[232:235], v[200:203], v[176:179], v[112:115]\n"
            MF "v[236:239], v[216:219], v[176:179], v[116:119]\n"
            MF "v[232:235], v[204:207], v[180:183], v[232:235]\n"
            MF "v[236:239], v[220:223], v[180:183], v[236:239]\n"
            MF "v[232:235], v[208:211], v[184:187], v[232:235]\n"
            MF "v[236:239], v[224:227], v[184:187], v[236:239]\n"
            "ds_read_b128 v[96:99], %[kra0]\nds_read_b128 v[100:103], %[kra1]\n"
            "ds_read_b128 v[104:107], %[kra0] offset:1024\nds_read_b128 v[108:111], %[kra1] offset:1024\n"
            MF "v[232:235], v[212:215], v[188:191], v[232:235]\n"
            MF "v[236:239], v[228:231], v[188:191], v[236:239]\n"
            "s_mul_i32 s61, %[s], 81920\n"
            "s_waitcnt lgkmcnt(0)\n"
            MF "v[240:243], v[96:99], v[176:179], 0\n"
            MF "v[240:243], v[100:103], v[180:183], v[240:243]\n"
            MF "v[240:243], v[104:107], v[184:187], v[240:243]\n"
            MF "v[240:243], v[108:111], v[188:191], v[240:243]\n"
            "v_max3_f32 v244, v232, v233, v234\n"
            "v_max3_f32 v245, v235, v236, v237\n"
            "v_max3_f32 v244, v244, v238, v239\n"
            "v_max_f32 v244, v244, v245\n"
            "s_nop 4\n"
            "v_max3_f32 v245, v240, v241, v242\n"
            "v_max3_f32 v244, v244, v245, v243\n"
            "v_mov_b32 v245, v244\n"
            "s_nop 1\n"
            "v_permlane16_swap_b32 v244, v245\n"
            "v_max_f32 v244, v244, v245\n"
            "v_mov_b32 v245, v244\n"
            "s_nop 1\n"
            "v_permlane32_swap_b32 v244, v245\n"
            "v_max_f32 v244, v244, v245\n"
            "v_mul_f32 v244, %[sc], v244\n"
            ".set wl_i, 232\n.rept 12\nv_fma_f32 v[wl_i], v[wl_i], %[sc], -v244\n.set wl_i, wl_i+1\n.endr\n"
            ".set wl_i, 232\n.rept 12\nv_exp_f32 v[wl_i], v[wl_i]\n.set wl_i, wl_i+1\n.endr\n"
            "v_pk_add_f32 v[246:247], v[232:233], v[234:235]\n"
            "v_pk_add_f32 v[248:249], v[236:237], v[238:239]\n"
            "v_pk_add_f32 v[244:245], v[240:241], v[242:243]\n"
            "v_pk_add_f32 v[246:247], v[246:247], v[248:249]\n"
            "v_add_f32 v244, v244, v245\n"
            "v_add_f32 v246, v246, v247\n"
            "v_fmac_f32 v246, 0.5, v244\n"
            "v_mov_b32 v247, v246\n"
            "s_nop 1\n"
            "v_permlane16_swap_b32 v246, v247\n"
            "v_add_f32 v246, v246, v247\n"
            "v_mov_b32 v247, v246\n"
            "s_nop 1\n"
            "v_permlane32_swap_b32 v246, v247\n"
            "v_add_f32 v246, v246, v247\n"
            "v_rcp_f32 v246, v246\n"
            "v_mov_b32 v244, %[s]\n"
            ".set wl_i, 232\n.rept 6\nv_pk_mul_f32 v[wl_i:wl_i+1], v[wl_i:wl_i+1], v[246:247] op_sel_hi:[1,0]\n.set wl_i, wl_i+2\n.endr\n"
            "v_cvt_pk_bf16_f32 v192, v232, v236\nv_cvt_pk_bf16_f32 v193, v233, v237\n"
            "v_cvt_pk_bf16_f32 v194, v234, v238\nv_cvt_pk_bf16_f32 v195, v235, v239\n"
            "v_cvt_pk_bf16_f32 v196, v240, v241\nv_cvt_pk_bf16_f32 v197, v242, v243\n"
            "buffer_store_dwordx4 v[192:195], %[pxo], %[srd], s61 offen sc1\n"
            "buffer_store_dwordx2 v[196:197], %[pmo], %[srd], s61 offen sc1\n"
            "s_cmp_eq_u32 %[s], 63\n"
            "s_cbranch_scc1 Lend%=\n"
            "s_cmp_ge_u32 %[s], 60\n"
            "s_cselect_b32 s61, 0, 0x40000000\n"
            "buffer_store_dword v244, %[fk], %[srd], s61 offen sc1\n" PT(3)
            QK_NEXT
            QZERO
            QK_TAIL(4)
            : [s] "=&s"(s), [so] "=&s"(so), [hm] "=&s"(hm), [bud] "+s"(budget), [seen] "+s"(seen), [pf] "+v"(pf), [tso] "+v"(tso)
            : [rw] "s"(rw), [wvo] "v"(wvo), [srd] "s"(srd), [mo] "v"(mo), [idx] "v"(idx), [ap1] "v"(ap1), [ap2] "v"(ap2), [ap3] "v"(ap3), [wa0] "v"(wa0), [wa1] "v"(wa1),
              [kra0] "v"(kra0), [kra1] "v"(kra1), [kxo] "v"(kxo), [pxo] "v"(pxo), [pmo] "v"(pmo), [kr] "v"(kr), [qr] "v"(qr), "{v[112:119]}"(cm), [i1] "s"(i1), [i2] "s"(i2),
              [gd] "s"(P.dly[0]), [gq] "s"(P.dly[8]), [gn] "s"(gn), [t0] "s"(t0), [fq] "s"(fq), [fk] "v"(fk), [sc] "s"(SC_LOG2), [w] "s"(w)
            : ACL, CLM, "v96", "v97", "v98", "v99", CL8(10), "v110", "v111", CL8(12), CL8(13), CL8(14), CL8(15), CL8(16), CL8(17), CL8(18), CL8(19), CL8(20), CL8(21),
              CL8(22), CL8(23), CL8(24), "v250", "v251", "v252", "v253", "v254", "v255", SCL, "vcc", "scc", "memory");
    }
    WAIT_VM0();
    BAR();
    if (threadIdx.x == 0) st32<SC1>(NSEG, rws, C_PF + h * 128, 0);
    if (budget < 0 && lane == 0) st32<SC1>(1, rws, C_ERR, 0);
    STAMP(128 + h);
    PDUMP(h);
}

#define V2(A, b0, b1) SMF "v[128:131], " A ", a[" b0 "], %[idx]\n" SMF "v[132:135], " A ", a[" b1 "], %[idx]\n"
#define VWL(r, o, so) "buffer_load_dwordx4 v[" r "], %[vwo], %[srd], " so " offen offset:" o " sc1\n"
#define UM(d, A, b) SMF "v[" d "], " A ", a[" b "], %[idx]\n"
#define F0 "v[168:171]"
#define F1 "v[172:175]"
#define SW(a, b) "v_permlane32_swap_b32 v" #a ", v" #b "\n"
#define S16(a, b) "v_permlane16_swap_b32 v" #a ", v" #b "\n"
#define AD(a, b) "v_add_f32 v" #a ", v" #a ", v" #b "\n"
#define CV(d, a, b) "v_cvt_pk_bf16_f32 v" #d ", v" #a ", v" #b "\n"
#define MN(d) "v_min_u32 v" #d ", -2, v" #d "\n"
#define VZERO Z4(128, 129, 130, 131) Z4(132, 133, 134, 135)
#define UZERO Z4(176, 177, 178, 179) Z4(180, 181, 182, 183) Z4(184, 185, 186, 187) Z4(188, 189, 190, 191) \
              Z4(192, 193, 194, 195) Z4(196, 197, 198, 199) Z4(200, 201, 202, 203) Z4(204, 205, 206, 207)
#define VWFMIN(r, d) \
    "v_min_u32_dpp v" #r ", v" #r ", v" #r " row_ror:4" DPPM "s_nop 1\n" \
    "v_min_u32_dpp v" #r ", v" #r ", v" #r " row_ror:2" DPPM "s_nop 1\n" \
    "v_min_u32_dpp v" #r ", v" #r ", v" #r " row_ror:1" DPPM "s_nop 0\n" \
    "v_readfirstlane_b32 " d ", v" #r "\n"
#define VO_FLAG(L) \
    "Lflag" L "%=:\n" \
    "buffer_load_dword v248, %[fvo], %[srd], 0 offen sc1\n" \
    "s_waitcnt vmcnt(0)\n" \
    VWFMIN(248, "%[seen]") \
    "s_cmp_gt_u32 %[seen], s69\n" \
    "s_cbranch_scc1 Lhave" L "%=\n" \
    "s_sub_u32 %[bud], %[bud], 1\n" \
    "s_cmp_lt_i32 %[bud], 0\n" \
    "s_cbranch_scc1 Lhave" L "%=\n" \
    "s_sleep 2\n" \
    "s_branch Lflag" L "%=\n" \
    "Lhave" L "%=:\n"
#define VO_VISSUE \
    "s_add_u32 s61, s59, wl_t/2\ns_and_b32 s61, s61, 3\ns_lshl_b32 s61, s61, 20\n" \
    ".if wl_t < 2\n.set wl_d, 96+wl_t*16\n.else\n.set wl_d, 96+wl_t*16\n.endif\n" \
    ".set wl_i, 0\n.rept 16\n" \
    "s_add_u32 s62, s61, ((wl_t&1)*4 + 2*(wl_i>>3) + 64*((wl_i>>1)&1) + 16*((wl_i>>2)&1) + 8*(wl_i&1))*8192\n" \
    "buffer_load_dword v[wl_d+wl_i], %[wvo], %[rv], s62 offen\n" \
    ".set wl_i, wl_i+1\n.endr\n.set wl_t, wl_t+1\n"
#define VO_WLOAD \
    "s_mov_b32 s63, 0x05040100\ns_mov_b32 s64, 0x07060302\n" \
    ".set wl_i, 0\n.rept 32\n" \
    "s_mov_b32 s62, (64*(wl_i>>4) + 32*((wl_i>>3)&1) + 8*((wl_i>>1)&1) + 2*((wl_i>>2)&1))*1024\n" \
    "buffer_load_dwordx4 v[128+wl_i*4:131+wl_i*4], %[woo], %[ro], s62 offen offset:(wl_i&1)*1024\n" \
    ".set wl_i, wl_i+1\n.endr\n" \
    ".set wl_t, 0\n" VO_VISSUE \
    ".set wl_p, 0\n.rept 2\n" \
    "s_waitcnt vmcnt(32)\n" VO_VISSUE \
    ".set wl_i, 0\n.rept 8\n.set wl_k, 0\n.rept 4\n" \
    ".set wl_b, 128+wl_p*64+wl_i*8+wl_k\n" \
    "v_perm_b32 v84, v[wl_b+4], v[wl_b], s63\nv_perm_b32 v85, v[wl_b+4], v[wl_b], s64\n" \
    "v_accvgpr_write_b32 a[128+wl_k*32+wl_p*8+wl_i], v84\nv_accvgpr_write_b32 a[128+wl_k*32+16+wl_p*8+wl_i], v85\n" \
    ".set wl_k, wl_k+1\n.endr\n.set wl_i, wl_i+1\n.endr\n" \
    ".set wl_p, wl_p+1\n.endr\n" \
    ".set wl_p, 0\n.rept 8\n" \
    ".if wl_p < 6\ns_waitcnt vmcnt(32)\n.elseif wl_p == 6\ns_waitcnt vmcnt(16)\n.else\ns_waitcnt vmcnt(0)\n.endif\n" \
    ".if wl_t < 8\n" VO_VISSUE ".endif\n" \
    ".set wl_i, 0\n.rept 8\n" \
    ".set wl_b, 96+wl_p*16+wl_i*2\n" \
    "v_perm_b32 v84, v[wl_b+1], v[wl_b], s63\nv_perm_b32 v85, v[wl_b+1], v[wl_b], s64\n" \
    "v_accvgpr_write_b32 a[wl_p*16+wl_i], v84\nv_accvgpr_write_b32 a[wl_p*16+8+wl_i], v85\n" \
    ".set wl_i, wl_i+1\n.endr\n.set wl_p, wl_p+1\n.endr\n"
DEV void role_vo(const Params& P, rsrc_t rws, u32x4 srd, char* lds, int h, int w, int lane) {
    const int g = lane >> 4, c = lane & 15;
    const u32x4 rv = mksrd(P.wv, 4u << 20), ro = mksrd(P.wo, 4u << 20);
    const unsigned wvo = ((g >> 1) + 32 * (g & 1)) * 8192 + h * 256 + (32 * w + 2 * c) * 2;
    const unsigned woo = (h * 128 + 16 * (g >> 1) + 4 * (g & 1)) * 1024 + (128 * w + 8 * c) * 2;
    int budget = SPIN_CAP, s;
    const int idx = (c < 8) ? 0x4444 : 0xEEEE;
    const int rho0 = 4 * (g & 1) + (g >> 1);
    const int vga = 2 * (w & 1) + (c >> 3);
    const unsigned va0 = 8192 + (w >> 1) * 1024 + (vga * 16 + ((c >> 2) & 1) * 8 + ((rho0 + 2 * (vga & 1)) & 7)) * 16 + (c & 3) * 4;
    const unsigned va1 = 8192 + (w >> 1) * 1024 + (vga * 16 + ((c >> 2) & 1) * 8 + ((rho0 + 2 + 2 * (vga & 1)) & 7)) * 16 + (c & 3) * 4;
    const unsigned vra = 8192 + (g * 16 + (c & 8) + (((c & 7) + 2 * (g & 1)) & 7)) * 16;
    const unsigned mo = w * 2048 + lane * 16;
    const unsigned ap1 = ((2 * w + 2) & 7) * 1024 + lane * 16, ap2 = ((2 * w + 4) & 7) * 1024 + lane * 16, ap3 = ((2 * w + 6) & 7) * 1024 + lane * 16;
    const unsigned uso = OFF_U + (8 * w + 2 * (g & 1) + (lane >> 5)) * 16384 + h * 256 + c * 16;
    unsigned so, hm;
    unsigned pf = 16384 + w * 2048;
    const unsigned t0 = (unsigned)__builtin_amdgcn_s_memrealtime();
    unsigned tso = lane < 32 ? OFF_TS + lane * 128 : OOR;
    const unsigned i1 = P.dly[3] * 3 / 4, i2 = (P.dly[13] - P.dly[3]) * 3 / 4;
    const unsigned gn = P.dly[0] - P.dly[2] + P.dly[4];
    asm volatile(
        "s_mov_b32 s59, %[w]\n"
        VO_WLOAD CLRWAIT(1) CLRSO
        "s_mov_b32 s72, s63\n"
        "s_mov_b32 %[s], 0\ns_mov_b32 s79, %[t0]\n" VZERO UZERO
        SETSO("%[so]", "%[s]")
        GRID MISSUE(136)
        "s_waitcnt vmcnt(5)\n"
        SMF "v[128:131], v[136:139], a[0:7], %[idx]\n"
        "ds_write_b128 %[mo], v[136:139]\n"
        "v_max3_u32 v248, v136, v137, v138\n"
        SMF "v[132:135], v[136:139], a[8:15], %[idx]\n"
        "s_waitcnt vmcnt(4)\n"
        "ds_write_b128 %[mo], v[140:143] offset:1024\n"
        "v_max3_u32 v249, v139, v140, v141\n"
        "v_max3_u32 v248, v248, v142, v143\n"
        "v_max_u32 v248, v248, v249\n"
        "v_cmp_eq_u32 vcc, -1, v248\n"
        "s_cbranch_vccnz Lretry%=\n"
        "Lgo%=:\n" PT(1)
        MREAD(144)
        V2("v[140:143]", "16:23", "24:31")
        "s_waitcnt lgkmcnt(5)\n" V2("v[144:147]", "32:39", "40:47")
        "s_waitcnt lgkmcnt(4)\n" V2("v[148:151]", "48:55", "56:63")
        "s_waitcnt lgkmcnt(3)\n" V2("v[152:155]", "64:71", "72:79")
        "s_waitcnt lgkmcnt(2)\n" V2("v[156:159]", "80:87", "88:95")
        "s_waitcnt lgkmcnt(1)\n" V2("v[160:163]", "96:103", "104:111")
        "s_waitcnt lgkmcnt(0)\n" V2("v[164:167]", "112:119", "120:127")
        "s_add_u32 s67, s63, 0x10000\ns_nop 2\n"
        SW(128, 129) SW(130, 131) AD(128, 129) AD(130, 131)
        SW(132, 133) SW(134, 135) AD(132, 133) CV(248, 128, 132)
        "ds_write_b32 %[va0], v248\n"
        AD(134, 135) CV(249, 130, 134)
        "ds_write_b32 %[va1], v249\n"
        "s_waitcnt lgkmcnt(0)\n"
        "s_barrier\n"
        "ds_read_b128 v[168:171], %[vra]\nds_read_b128 v[172:175], %[vra] offset:1024\n"
        VZERO
        "s_waitcnt lgkmcnt(1)\n"
        UM("176:179", F0, "128:135") UM("180:183", F0, "144:151")
        "s_waitcnt lgkmcnt(0)\n"
        UM("176:179", F1, "136:143") UM("180:183", F1, "152:159")
        UM("184:187", F0, "160:167") UM("188:191", F0, "176:183")
        SW(176, 180) SW(177, 181) SW(178, 182)
        UM("184:187", F1, "168:175")
        SW(179, 183) AD(176, 180) AD(177, 181)
        UM("188:191", F1, "184:191")
        AD(178, 182) AD(179, 183) CV(240, 176, 177)
        UM("192:195", F0, "192:199")
        CV(241, 178, 179) MN(240) MN(241)
        UM("196:199", F0, "208:215")
        SW(184, 188) SW(185, 189) SW(186, 190)
        UM("192:195", F1, "200:207")
        SW(187, 191) AD(184, 188) AD(185, 189)
        UM("196:199", F1, "216:223")
        AD(186, 190) AD(187, 191) CV(242, 184, 185)
        UM("200:203", F0, "224:231")
        CV(243, 186, 187) MN(242) MN(243)
        UM("204:207", F0, "240:247")
        SW(192, 196) S16(240, 242) S16(241, 243)
        UM("200:203", F1, "232:239")
        "buffer_store_dwordx4 v[240:243], %[uso], %[srd], s63 offen sc1\n"
        SW(193, 197) SW(194, 198)
        UM("204:207", F1, "248:255")
        SW(195, 199) AD(192, 196) AD(193, 197) AD(194, 198) AD(195, 199) CV(244, 192, 193) CV(245, 194, 195) MN(244)
        SW(200, 204) SW(201, 205) SW(202, 206) SW(203, 207) AD(200, 204) AD(201, 205) AD(202, 206) AD(203, 207)
        CV(246, 200, 201) CV(247, 202, 203) MN(246) MN(247) MN(245)
        S16(244, 246) "s_nop 0\n" S16(245, 247)
        "buffer_store_dwordx4 v[244:247], %[uso], %[srd], s67 offen sc1\n" PT(2)
        "s_add_u32 s63, s63, 0x2000000\ns_add_u32 s67, s67, 0x2000000\n"
        "buffer_store_dwordx4 v[240:243], %[uso], %[srd], s63 offen sc1\n"
        "buffer_store_dwordx4 v[244:247], %[uso], %[srd], s67 offen sc1\n"
        "s_cmp_eq_u32 %[s], 63\n"
        "s_cbranch_scc1 Lend%=\n"
        TSLOAD
        UZERO
        TSFOLLOW(0)
        PFLUSH
        "s_add_u32 %[s], %[s], 1\n"
        SETSO("%[so]", "%[s]")
        BIGSO("s63", "%[s]", 19, 2)
        "s_branch Lpoll%=\n"
        MRETRY(136)
        "Lend%=:\ns_waitcnt vmcnt(0)\n"
        : [s] "=&s"(s), [so] "=&s"(so), [hm] "=&s"(hm), [bud] "+s"(budget), [pf] "+v"(pf), [tso] "+v"(tso)
        : [rv] "s"(rv), [ro] "s"(ro), [wvo] "v"(wvo), [woo] "v"(woo),
          [srd] "s"(srd), [mo] "v"(mo), [idx] "v"(idx), [ap1] "v"(ap1), [ap2] "v"(ap2), [ap3] "v"(ap3), [va0] "v"(va0), [va1] "v"(va1),
          [vra] "v"(vra), [uso] "v"(uso), [i1] "s"(i1), [i2] "s"(i2),
          [gd] "s"(P.dly[0]), [gq] "s"(P.dly[8]), [gn] "s"(gn), [t0] "s"(t0), [w] "s"(w)
        : ACL, CLM, "v96", "v97", "v98", "v99", CL8(10), CL8(11), CL8(12), CL8(13), CL8(14), CL8(15), CL8(16), CL8(17), CL8(18), CL8(19), CL8(20), CL8(21),
          CL8(22), CL8(23), CL8(24), "v250", "v251", "v252", "v253", "v254", "v255", "s59", SCL, "vcc", "scc", "memory");
    if (budget < 0 && lane == 0) st32<SC1>(2, rws, C_ERR, 0);
    STAMP(160 + h);
    PDUMP(32 + h);
}

#define VWALL VWL("96:99", "0", "s61") VWL("100:103", "1024", "s61") VWL("104:107", "2048", "s61") VWL("108:111", "3072", "s61") \
              VWL("112:115", "0", "s62") VWL("116:119", "1024", "s62") VWL("120:123", "2048", "s62") VWL("124:127", "3072", "s62")
#define OWP(n, r, b) \
    "s_waitcnt vmcnt(" #n ")\n" \
    SMF "v[236:239], v[" #r ":" #r "+3], v[" b "], %[idx]\n" \
    "v_max3_u32 v248, v248, v[" #r "], v[" #r "+1]\nv_max3_u32 v249, v249, v[" #r "+2], v[" #r "+3]\n"
#define OWB(r, u) \
    OWP(4, r, "96:103") OWP(3, r+4, "104:111") OWP(2, r+8, "112:119") OWP(1, r+12, "120:127") OWP(0, r+16, u) \
    "v_max_u32 v248, v248, v249\n" \
    "v_cmp_eq_u32 vcc, -1, v248\n"
#define OWC(a, so) \
    "buffer_load_dwordx4 v[168:171], %[ulo], %[srd], " so " offen sc1\n" \
    "buffer_load_dwordx4 v[172:175], %[ulo], %[srd], " so " offen offset:1024 sc1\n" \
    "buffer_load_dwordx4 v[148:151], v[" #a "], %[srd], %[so] offen sc1\n" \
    "buffer_load_dwordx4 v[152:155], v[" #a "+1], %[srd], %[so] offen sc1\n" \
    "buffer_load_dwordx4 v[156:159], v[" #a "+2], %[srd], %[so] offen sc1\n" \
    "buffer_load_dwordx4 v[160:163], v[" #a "+3], %[srd], %[so] offen sc1\n" \
    "buffer_load_dwordx4 v[164:167], v[" #a "+4], %[srd], %[so] offen sc1\n"
#define OWD \
    "s_waitcnt vmcnt(5)\n" \
    "v_max3_u32 v248, v168, v169, v170\nv_max3_u32 v249, v171, v172, v173\nv_max3_u32 v248, v248, v174, v175\n" \
    Z4(236, 237, 238, 239) \
    S16(168, 170) S16(169, 171) S16(172, 174) S16(173, 175) \
    "v_swap_b32 v169, v170\nv_swap_b32 v173, v174\n" \
    OWB(148, "168:175")
DEV void role_ow(const Params& P, rsrc_t rws, u32x4 srd, char* lds, int h, int w, int lane) {
    const int g = lane >> 4, c = lane & 15;
    int budget = SPIN_CAP, s;
    const int idx = (c < 8) ? 0x4444 : 0xEEEE;
    const unsigned ulo = OFF_U + h * 16384 + w * 2048 + lane * 16;
    u32x8 pl, pb, pc;
    {
        const int hf = (c >> 3), r = c & 7;
#pragma unroll
        for (int n = 0; n < 5; ++n) {
            unsigned o = n < 4 ? (8 * w + 2 * n + (g >> 1)) * 640 + (g & 1) * 256 + hf * 128 + r * 16
                               : (8 * w + 2 * g + hf) * 640 + 512 + r * 16;
            pl[n] = pofs(o); pb[n] = pofs(o + 20480); pc[n] = pofs(o + 40960);
        }
        pb[5] = pb[6] = pb[7] = pc[5] = pc[6] = pc[7] = 0;
        pl[5] = w * 512 + ((g >> 1) * 16 + (g & 1) * 32 + c) * 4;
        const int rr = (lane >> 2) & 7;
        pl[6] = ((rr & 1) * 16 + (rr >> 2) * 32 + ((rr >> 1) & 1) * 64 + 8 * (lane >> 5) + 2 * (lane & 3)) * 4;
        pl[7] = h * 256 + lane * 4 + (w ? w - 1 : 0) * 16384;
    }
    const unsigned vwo = OFF_VW + h * 65536 + w * 16384 + lane * 16;
    const unsigned fvo = C_VWF + ((h >> 3) * 32 + w * 8 + (lane & 7)) * 128;
    unsigned so, hm;
    unsigned pf = 16384 + w * 2048;
    const unsigned t0 = (unsigned)__builtin_amdgcn_s_memrealtime();
    unsigned tso = lane < 32 ? OFF_TS + lane * 128 : OOR, tsw = lane == 0 ? OFF_TS + h * 128 : OOR;
    unsigned seen = 0;
    asm volatile(
        CLRWAIT(1) CLRSO
        "s_mov_b32 s72, s63\nv_add_u32 %[tsw], s62, %[tsw]\n"
        "s_mov_b32 s69, 0\n"
        VO_FLAG("A")
        "s_mov_b32 %[s], 0\ns_mov_b32 s79, %[t0]\n" Z4(236, 237, 238, 239)
        SETSO("%[so]", "%[s]")
        "s_xor_b32 s61, s72, 0x2000\ns_add_u32 s62, s61, 0x1000\n"
        VWALL
        "s_mov_b32 s68, 1\n" SETSO("s70", "s68")
        "s_memrealtime s[76:77]\ns_waitcnt lgkmcnt(0)\ns_mov_b32 s79, s76\ns_mov_b32 s74, s76\ns_mov_b32 s82, 0\n"
        GRID
        OWC(88, "s63") "s_mov_b32 s75, 0\n" OWD
        "s_cbranch_vccnz Lretry%=\n"
        "Lgo%=:\n" PT(4)
        "s_cmp_eq_u32 %[w], 1\n"
        "s_nop 1\n"
        SW(236, 237) SW(238, 239) AD(236, 237) AD(238, 239)
        "ds_write2_b32 v93, v236, v238 offset1:64\n"
        "s_waitcnt lgkmcnt(0)\n"
        "s_barrier\n"
        "s_cbranch_scc1 Lnom%=\n"
        "ds_read2_b64 v[216:219], v94 offset1:64\n"
        "ds_read2_b64 v[220:223], v94 offset0:128 offset1:192\n"
        "s_waitcnt lgkmcnt(1)\n"
        "v_pk_add_f32 v[216:217], v[216:217], v[218:219]\n"
        "s_waitcnt lgkmcnt(0)\n"
        "v_pk_add_f32 v[220:221], v[220:221], v[222:223]\n"
        "v_pk_add_f32 v[216:217], v[216:217], v[220:221]\n"
        CV(216, 216, 217) MN(216)
        "buffer_store_dword v216, v95, %[srd], s70 offen sc1\n"
        "s_add_u32 s60, s70, 0xc000\n"
        "buffer_store_dword v216, v95, %[srd], s60 offen sc1\n"
        "Lnom%=:\n" PT(5)
        "s_cmp_eq_u32 %[s], 62\n"
        "s_cbranch_scc1 Lend%=\n"
        "s_cmp_lg_u32 %[w], 1\n"
        "s_cbranch_scc1 Lnot%=\n"
        "s_memrealtime s[76:77]\n"
        "s_add_u32 s60, s74, %[gd]\n"
        "s_add_u32 s65, s74, %[th]\n"
        "s_waitcnt lgkmcnt(0)\n"
        "s_sub_i32 s78, s76, s65\n"
        "s_cmp_gt_i32 s78, " STR(XFAR) "\n"
        "s_cselect_b32 s65, %[gx], %[gy]\n"
        "s_add_u32 s76, s76, s65\n"
        "s_cmp_gt_i32 s78, 0\n"
        "s_cselect_b32 s65, %[q1], 0\n"
        "s_cmp_gt_i32 s78, %[d1]\n"
        "s_cselect_b32 s65, %[q2], s65\n"
        "s_add_u32 s60, s60, s65\n"
        "s_cmp_gt_i32 s78, %[d2]\n"
        "s_cselect_b32 s60, s76, s60\n"
        "s_min_u32 s60, s60, -2\n"
        "s_lshl_b32 s65, s68, 14\n"
        "v_mov_b32 v216, s60\n"
        "buffer_store_dword v216, %[tsw], %[srd], s65 offen sc1\n"
        "Lnot%=:\n"
        Z4(236, 237, 238, 239)
        PFLUSH
        "s_add_u32 %[s], %[s], 1\n"
        "s_mov_b32 %[so], s70\n"
        "s_mov_b32 s69, %[s]\n"
        BIGSO("s61", "s69", 21, 0)
        "s_xor_b32 s61, s61, 0x2000\n"
        "s_add_u32 s62, s61, 0x1000\n"
        "s_mov_b32 s79, s74\n"
        WAITT("%[dr]", "Lv") PT(2)
        TSLOAD
        "buffer_load_dword v252, off, %[srd], %[fra] sc1\n"
        "s_add_u32 s60, %[s], " STR(LEAD) "\ns_min_u32 s60, s60, 64\n"
        "s_cmp_lt_u32 %[seen], s60\n"
        "s_cselect_b32 s60, 0, 0x40000000\n"
        "buffer_load_dword v253, %[fvo], %[srd], s60 offen sc1\n"
        "s_cmp_gt_u32 %[seen], s69\n"
        "s_cbranch_scc1 LhaveB%=\n"
        VO_FLAG("B")
        VWALL
        BIGSO("s63", "%[s]", 19, 2)
        "s_add_u32 s68, %[s], 1\n" SETSO("s70", "s68")
        "s_add_u32 s79, s79, %[gd]\n"
        "s_waitcnt vmcnt(8)\n"
        VWFMIN(253, "s60")
        "s_max_u32 %[seen], %[seen], s60\n"
        "Lra%=:\n"
        "v_readfirstlane_b32 s60, v252\n"
        "s_add_u32 s60, s60, 3\n"
        "s_cmp_gt_u32 s60, %[s]\n"
        "s_cbranch_scc1 Lrad%=\n"
        "s_sleep 2\n"
        "buffer_load_dword v252, off, %[srd], %[fra] sc1\n"
        "s_sub_u32 %[bud], %[bud], 1\n"
        "s_waitcnt vmcnt(0)\n"
        "s_cmp_lt_i32 %[bud], 0\n"
        "s_cbranch_scc0 Lra%=\n"
        "Lrad%=:\n"
        TSMAX(8) PT(3)
        "s_cbranch_vccz Ltsok%=\n"
        WAITT("-45", "Lt")
        "Lts%=:\n"
        TSLOAD TSMAX(0)
        "s_cbranch_vccz Ltsok%=\n"
        "s_sub_u32 %[bud], %[bud], 1\n"
        "s_cmp_lt_i32 %[bud], 0\n"
        "s_cbranch_scc0 Lts%=\n"
        "Ltsok%=:\n"
        "s_max_i32 s76, s76, 0\n"
        "s_add_u32 s79, s79, s76\n"
        "s_mov_b32 s74, s79\n"
        "s_branch Lpoll%=\n"
        "Lretry%=:\n"
        "s_add_u32 s60, s63, 0x2000000\n"
        OWC(80, "s60") "s_mov_b32 s75, 1\n" OWD
        "s_cbranch_vccz Lgo%=\n"
        OWC(72, "s63") "s_mov_b32 s75, 2\n" OWD
        "s_cbranch_vccz Lgo%=\n"
        "s_sub_u32 %[bud], %[bud], 1\n"
        "s_cmp_lt_i32 %[bud], 0\n"
        "s_cbranch_scc1 Lgo%=\n"
        "s_add_u32 s79, s79, %[gq]\n"
        "s_nop 7\n" Z4(236, 237, 238, 239)
        "s_branch Lpoll%=\n"
        "Lend%=:\ns_waitcnt vmcnt(0)\n"
        : [s] "=&s"(s), [so] "=&s"(so), [hm] "=&s"(hm), [bud] "+s"(budget), [seen] "+s"(seen), [pf] "+v"(pf), [tso] "+v"(tso), [tsw] "+v"(tsw)
        : "{v[88:95]}"(pl), "{v[80:87]}"(pb), "{v[72:79]}"(pc), [d1] "s"(P.dly[3] + P.dly[15]), [d2] "s"(P.dly[13] + 2 * P.dly[15]), [q1] "s"(P.dly[3] + P.dly[15] + P.dly[16]), [q2] "s"(P.dly[13] + 2 * P.dly[15] + P.dly[16]), [srd] "s"(srd), [idx] "v"(idx), [vwo] "v"(vwo), [fvo] "v"(fvo), [ulo] "v"(ulo),
          [gd] "s"(P.dly[0]), [gq] "s"(P.dly[1]), [dr] "s"(P.dly[6]), [gx] "s"(P.dly[7]), [gy] "s"(P.dly[10]), [th] "s"(P.dly[0] - P.dly[2] - P.dly[12]), [fra] "s"(C_U2F + h * 128),
          [t0] "s"(t0), [w] "s"(w)
        : "v96", "v97", "v98", "v99", CL8(10), CL8(11), CL8(12), CL8(13), CL8(14), CL8(15), CL8(16), CL8(17), CL8(20), CL8(21),
          CL8(22), CL8(23), CL8(24), "v250", "v251", "v252", "v253", "v254", "v255", SCL, "vcc", "scc", "memory");
    if (budget < 0 && lane == 0) st32<SC1>(3, rws, C_ERR, 0);
    STAMP(320 + h);
    PDUMP(64 + h);
}

#define XDMA(o) "buffer_load_dwordx4 %[xo], %[srd], s66 offen offset:" #o " sc1 lds\n"
#define XDMA8 "s_nop 0\n" XDMA(0) XDMA(1024) XDMA(2048) XDMA(3072) "s_add_u32 m0, m0, 0x1000\ns_add_u32 s66, s66, 0x1000\n" XDMA(0) XDMA(1024) XDMA(2048) XDMA(3072)
#define KQ_WISSUE \
    ".set wl_i, 0\n.rept 16\n" \
    "s_mov_b32 s62, (64*wl_t + 32*(wl_i>>3) + (wl_i&7))*8192\n" \
    "buffer_load_dwordx2 v[128+(wl_t&3)*32+wl_i*2:129+(wl_t&3)*32+wl_i*2], %[wvo], %[rw], s62 offen\n" \
    ".set wl_i, wl_i+1\n.endr\n.set wl_t, wl_t+1\n"
#define KQ_CVT(a, b) \
    "v_cvt_pk_bf16_f32 v[112+kq_t*8+kq_h*4+" #a "], v[64+kq_t*16+(" #a "&1)*8+2*kq_h+(" #a ">>1)], v[68+kq_t*16+(" #a "&1)*8+2*kq_h+(" #a ">>1)]\n" \
    "v_cvt_pk_bf16_f32 v[112+kq_t*8+kq_h*4+" #b "], v[64+kq_t*16+(" #b "&1)*8+2*kq_h+(" #b ">>1)], v[68+kq_t*16+(" #b "&1)*8+2*kq_h+(" #b ">>1)]\n"
#define KQ_ST(so) "buffer_store_dwordx4 v[112+kq_t*8+kq_h*4:115+kq_t*8+kq_h*4], %[oo], %[srd], " so " offen offset:kq_h*1024 sc1\n"
#define KQ_MF(b) \
    ".if (kq_n & 15) == 0\n" MF "v[64+(kq_n>>4)*16+" #b "*4:67+(kq_n>>4)*16+" #b "*4], a[" #b "*4:" #b "*4+3], v[96+(kq_n&3)*4:99+(kq_n&3)*4], 0\n.else\n" \
    MF "v[64+(kq_n>>4)*16+" #b "*4:67+(kq_n>>4)*16+" #b "*4], a[(kq_n&15)*16+" #b "*4:(kq_n&15)*16+" #b "*4+3], v[96+(kq_n&3)*4:99+(kq_n&3)*4], v[64+(kq_n>>4)*16+" #b "*4:67+(kq_n>>4)*16+" #b "*4]\n.endif\n"
DEV void role_kq(const Params& P, rsrc_t rws, u32x4 srd, int h, int w, int lane) {
    const int g = lane >> 4, c = lane & 15;
    const u32x4 rw = mksrd(w < 2 ? P.wk : P.wq, 4u << 20);
    const unsigned wvo = 8 * g * 8192 + h * 256 + (64 * (w & 1) + 4 * c) * 2;
    const unsigned xo = OFF_XN + w * 8192 + lane * 16, l16 = lane * 16;
    const unsigned oo = (P.rolemask & 32) ? OOR : (w < 2 ? OFF_KF : OFF_QF) + h * 16384 + (w & 1) * 2048 + lane * 16;
    const unsigned fo = lane == 0 ? C_KQF + h * 128 + w * 4 : OOR;
    int budget = SPIN_CAP;
    asm volatile(
        "s_mov_b32 s63, 0x05040100\ns_mov_b32 s64, 0x07060302\n"
        ".set wl_t, 0\n.rept 3\n" KQ_WISSUE ".endr\n"
        ".set wl_p, 0\n.rept 8\n"
        ".if wl_p < 6\ns_waitcnt vmcnt(32)\n.elseif wl_p == 6\ns_waitcnt vmcnt(16)\n.else\ns_waitcnt vmcnt(0)\n.endif\n"
        ".if wl_t < 8\n" KQ_WISSUE ".endif\n"
        ".set wl_i, 0\n.rept 8\n"
        ".set wl_b, 128+(wl_p&3)*32+wl_i*4\n.set wl_d, (2*wl_p+(wl_i>>2))*16+(wl_i&3)\n"
        "v_perm_b32 v120, v[wl_b+2], v[wl_b], s63\nv_perm_b32 v121, v[wl_b+2], v[wl_b], s64\n"
        "v_perm_b32 v122, v[wl_b+3], v[wl_b+1], s63\nv_perm_b32 v123, v[wl_b+3], v[wl_b+1], s64\n"
        "v_accvgpr_write_b32 a[wl_d], v120\nv_accvgpr_write_b32 a[wl_d+4], v121\n"
        "v_accvgpr_write_b32 a[wl_d+8], v122\nv_accvgpr_write_b32 a[wl_d+12], v123\n"
        ".set wl_i, wl_i+1\n.endr\n.set wl_p, wl_p+1\n.endr\n"
        CLRWAIT(0)
        "s_lshl_b32 s68, %[w], 13\n"
        "s_mov_b32 m0, s68\ns_mov_b32 s66, 0\n" XDMA8
        "s_add_u32 m0, s68, 0x8000\ns_mov_b32 s66, 0x8000\n" XDMA8
        "s_mov_b32 s69, 0\ns_mov_b32 s70, 0x40000000\ns_xor_b32 s72, s63, 0x2000\ns_mov_b32 s71, s72\n"
        "v_mov_b32 v60, %[l16]\nv_add_u32 v61, 0x8000, %[l16]\n"
        "s_waitcnt vmcnt(8)\ns_barrier\n"
        "ds_read_b128 v[96:99], v60\nds_read_b128 v[100:103], v60 offset:1024\nds_read_b128 v[104:107], v60 offset:2048\n"
        ".p2align 6\nLloop%=:\n"
        ".set kq_n, 0\n.rept 32\n"
        ".if kq_n == 29\ns_barrier\n.endif\n"
        "s_waitcnt lgkmcnt(2)\n"
        KQ_MF(0)
        ".if kq_n < 29\nds_read_b128 v[96+((kq_n+3)&3)*4:99+((kq_n+3)&3)*4], v60 offset:(kq_n+3)*1024\n"
        ".else\nds_read_b128 v[96+((kq_n+3)&3)*4:99+((kq_n+3)&3)*4], v61 offset:(kq_n-29)*1024\n.endif\n"
        KQ_MF(1)
        ".if kq_n == 1 || kq_n == 2\n.set kq_t, 1\n.set kq_h, kq_n-1\n" KQ_CVT(0, 1) ".endif\n"
        ".if kq_n == 18 || kq_n == 19\n.set kq_t, 0\n.set kq_h, kq_n-18\n" KQ_CVT(0, 1) ".endif\n"
        ".if kq_n == 3\ns_add_u32 s66, s69, 2\ns_and_b32 s67, s66, 3\ns_lshl_b32 s67, s67, 15\n.endif\n"
        ".if kq_n == 8\ns_add_u32 m0, m0, 0x1000\ns_add_u32 s66, s66, 0x1000\n.endif\n"
        ".if kq_n == 14\nv_mov_b32 v62, s69\n.endif\n"
        KQ_MF(2)
        ".if kq_n == 1 || kq_n == 2 || kq_n == 18 || kq_n == 19\n" KQ_CVT(2, 3) ".endif\n"
        ".if kq_n == 3\ns_add_u32 m0, s67, s68\ns_lshl_b32 s66, s66, 15\n.endif\n"
        ".if kq_n >= 4 && kq_n < 12\nbuffer_load_dwordx4 %[xo], %[srd], s66 offen offset:(kq_n&3)*1024 sc1 lds\n.endif\n"
        ".if kq_n == 14\ns_waitcnt vmcnt(8)\nbuffer_store_dword v62, %[fo], %[srd], 0 offen sc1\n.endif\n"
        KQ_MF(3)
        ".if kq_n == 1 || kq_n == 2\n" KQ_ST("s70") ".endif\n"
        ".if kq_n == 18 || kq_n == 19\n" KQ_ST("s71") ".endif\n"
        ".if kq_n == 30\ns_add_u32 s70, s71, 0x1000\ns_add_u32 s69, s69, 1\n" BIGSO("s71", "s69", 19, 2) ".endif\n"
        ".if kq_n == 31\ns_add_u32 s67, s69, 1\ns_and_b32 s67, s67, 3\ns_lshl_b32 s67, s67, 15\n.endif\n"
        ".set kq_n, kq_n+1\n.endr\n"
        "v_mov_b32 v60, v61\n"
        "v_add_u32 v61, s67, %[l16]\n"
        "s_cmp_lt_u32 s69, 64\n"
        "s_cbranch_scc1 Lloop%=\n"
        "s_nop 7\n"
        ".set kq_t, 1\n.set kq_h, 0\n" KQ_CVT(0, 1) KQ_CVT(2, 3) KQ_ST("s70")
        ".set kq_h, 1\n" KQ_CVT(0, 1) KQ_CVT(2, 3) KQ_ST("s70")
        "v_mov_b32 v62, 64\n"
        "s_waitcnt vmcnt(0) lgkmcnt(0)\n"
        "buffer_store_dword v62, %[fo], %[srd], 0 offen sc1\n"
        : [bud] "+s"(budget)
        : [rw] "s"(rw), [wvo] "v"(wvo), [srd] "s"(srd), [xo] "v"(xo), [l16] "v"(l16), [oo] "v"(oo), [fo] "v"(fo), [w] "s"(w)
        : ACL, CL8(6), CL8(7), CL8(8), CL8(9), CL8(10), CL8(11), CL8(12), CL8(13), CL8(14), CL8(15), CL8(16), CL8(17), CL8(18), CL8(19), CL8(20), CL8(21),
          CL8(22), CL8(23), CL8(24), "v250", "v251", "v252", "v253", "v254", "v255", SCL, "vcc", "scc", "memory");
    if (budget < 0 && lane == 0) st32<SC1>(5, rws, C_ERR, 0);
    STAMP(192 + h);
}

#define VW_VISSUE \
    ".set wl_i, 0\n.rept 16\n" \
    "s_mov_b32 s62, (64*wl_t + 32*(wl_i>>3) + (wl_i&7))*8192\n" \
    "buffer_load_dword v[96+wl_t*16+wl_i], %[wvo], %[rv], s62 offen\n" \
    ".set wl_i, wl_i+1\n.endr\n.set wl_t, wl_t+1\n"
#define VW_CVT(a, b) \
    "v_cvt_pk_bf16_f32 v[224+(vw_i&3)*4+" #a "], v[160+vw_i*8+" #a "], v[164+vw_i*8+" #a "]\n" \
    "v_cvt_pk_bf16_f32 v[224+(vw_i&3)*4+" #b "], v[160+vw_i*8+" #b "], v[164+vw_i*8+" #b "]\n"
#define VW_ST(so) "s_add_u32 s72, " so ", vw_i*0x10000\nbuffer_store_dwordx4 v[224+(vw_i&3)*4:227+(vw_i&3)*4], %[oo], %[srd], s72 offen sc1\n"
#define VW_VC(t) \
    "v_cvt_pk_bf16_f32 v[240+" #t "*4], v[112+" #t "*8], v[116+" #t "*8]\nv_cvt_pk_bf16_f32 v[241+" #t "*4], v[113+" #t "*8], v[117+" #t "*8]\n" \
    "v_cvt_pk_bf16_f32 v[242+" #t "*4], v[114+" #t "*8], v[118+" #t "*8]\nv_cvt_pk_bf16_f32 v[243+" #t "*4], v[115+" #t "*8], v[119+" #t "*8]\n" \
    "ds_write_b128 v62, v[240+" #t "*4:243+" #t "*4] offset:" #t "*4096\n"
#define VW_MV(b) \
    ".if (vw_n & 15) == 0\n" MF "v[112+(vw_n>>4)*8+" #b "*4:115+(vw_n>>4)*8+" #b "*4], a[" #b "*4:" #b "*4+3], v[96+(vw_n&3)*4:99+(vw_n&3)*4], 0\n.else\n" \
    MF "v[112+(vw_n>>4)*8+" #b "*4:115+(vw_n>>4)*8+" #b "*4], a[(vw_n&15)*8+" #b "*4:(vw_n&15)*8+" #b "*4+3], v[96+(vw_n&3)*4:99+(vw_n&3)*4], v[112+(vw_n>>4)*8+" #b "*4:115+(vw_n>>4)*8+" #b "*4]\n.endif\n"
#define VW_MO \
    ".if vw_k == 0\n" MF "v[160+vw_a*8+vw_t*4:163+vw_a*8+vw_t*4], v[128+vw_t*4:131+vw_t*4], a[128+vw_a*16:131+vw_a*16], 0\n.else\n" \
    MF "v[160+vw_a*8+vw_t*4:163+vw_a*8+vw_t*4], v[128+vw_k*8+vw_t*4:131+vw_k*8+vw_t*4], a[128+vw_a*16+vw_k*4:131+vw_a*16+vw_k*4], v[160+vw_a*8+vw_t*4:163+vw_a*8+vw_t*4]\n.endif\n"
#define VW_VF(k) "ds_read_b128 v[128+" #k "*8:131+" #k "*8], v63 offset:" #k "*1024\nds_read_b128 v[132+" #k "*8:135+" #k "*8], v63 offset:4096+" #k "*1024\n"
#define VW_BODY \
    ".set vw_n, 0\n.rept 32\n" \
    ".if vw_n < 3\ns_waitcnt lgkmcnt(4)\n.elseif vw_n < 30\ns_waitcnt lgkmcnt(2)\n.elseif vw_n == 30\ns_waitcnt lgkmcnt(1)\n.else\ns_waitcnt lgkmcnt(0)\n.endif\n" \
    VW_MV(0) \
    ".if vw_n < 29\nds_read_b128 v[96+((vw_n+3)&3)*4:99+((vw_n+3)&3)*4], v60 offset:(vw_n+3)*1024\n.endif\n" \
    ".if vw_f == 0\n" \
    ".if vw_n >= 1 && vw_n <= 8\n.set vw_i, 4+((vw_n-1)>>1)\n.elseif vw_n == 10 || vw_n == 11\n.set vw_i, 0\n.elseif vw_n == 18 || vw_n == 19\n.set vw_i, 1\n" \
    ".elseif vw_n == 26 || vw_n == 27\n.set vw_i, 2\n.else\n.set vw_i, -1\n.endif\n" \
    ".if vw_n <= 8\n.set vw_e, 1-(vw_n&1)\n.else\n.set vw_e, vw_n&1\n.endif\n" \
    ".if vw_i >= 0 && vw_e == 0\n" VW_CVT(0, 1) ".endif\n" \
    ".set vw_a, vw_n>>3\n.set vw_k, (vw_n>>1)&3\n.set vw_t, vw_n&1\n" VW_MO \
    ".if vw_i >= 0 && vw_e == 0\n" VW_CVT(2, 3) ".endif\n" \
    ".if vw_i >= 4 && vw_e != 0\n" VW_ST("s70") ".endif\n" \
    ".if vw_i >= 0 && vw_i < 4 && vw_e != 0\n" VW_ST("s71") ".endif\n" \
    ".if vw_n == 21\nv_mov_b32 v248, s69\ns_waitcnt vmcnt(10)\nbuffer_store_dword v248, %[fo], %[srd], 0 offen sc1\n.endif\n" \
    ".endif\n" \
    ".if vw_n == 9\ns_mov_b32 m0, s73\n.endif\n" \
    ".if vw_n == 16\ns_add_u32 m0, m0, 0x1000\ns_add_u32 s66, s66, 0x1000\n.endif\n" \
    VW_MV(1) \
    ".if vw_n >= 12 && vw_n < 20\nbuffer_load_dwordx4 %[xo], %[srd], s66 offen offset:(vw_n&3)*1024 sc1 lds\n.endif\n" \
    ".if vw_n == 18\n" VW_VC(0) ".endif\n" \
    ".set vw_n, vw_n+1\n.endr\n" \
    ".set vw_n, 0\n.rept 32\n" \
    ".set vw_a, 4+((vw_n>>1)&3)\n.set vw_k, vw_n>>3\n.set vw_t, vw_n&1\n" \
    ".if vw_f == 0\n" VW_MO ".endif\n" \
    ".if vw_n == 1\ns_nop 5+2*vw_f\n.endif\n" \
    ".if vw_n == 2\n" VW_VC(1) ".endif\n" \
    ".if vw_n == 3 && vw_f == 0\n.set vw_i, 3\n" VW_CVT(0, 1) VW_CVT(2, 3) ".endif\n" \
    ".if vw_n == 4 && vw_f == 0\n" VW_ST("s71") ".endif\n" \
    ".if vw_n == 5\n.if vw_f\ns_waitcnt vmcnt(8)\n.endif\ns_waitcnt lgkmcnt(0)\ns_barrier\n.endif\n" \
    ".if vw_n == 8\n" VW_VF(0) ".endif\n.if vw_n == 16\n" VW_VF(1) ".endif\n.if vw_n == 24\n" VW_VF(2) ".endif\n" \
    ".if vw_n == 10\ns_mov_b32 s70, s71\ns_add_u32 s71, s71, 0x200000\ns_xor_b32 s71, s71, 0x2000\ns_add_u32 s69, s69, 1\n.endif\n" \
    ".if vw_n == 11\ns_mov_b32 s73, s74\ns_mov_b32 s74, s75\ns_sub_u32 s75, m0, 0x1000\n.endif\n" \
    ".if vw_n == 12\ns_add_u32 s66, s66, 0x7000\ns_sub_u32 s67, s74, s68\n.endif\n" \
    ".if vw_n == 13\nv_add_u32 v60, s67, %[l16]\nv_xor_b32 v62, 0x2000, v62\n.endif\n" \
    ".if vw_n == 26 || vw_n == 28 || vw_n == 30\nds_read_b128 v[96+((vw_n-26)>>1)*4:99+((vw_n-26)>>1)*4], v60 offset:((vw_n-26)>>1)*1024\n.endif\n" \
    ".set vw_n, vw_n+1\n.endr\n" \
    VW_VF(3) \
    "v_xor_b32 v63, 0x2000, v63\n"
DEV void role_vw(const Params& P, rsrc_t rws, u32x4 srd, int h, int w, int lane) {
    const int g = lane >> 4, c = lane & 15;
    const u32x4 rv = mksrd(P.wv, 4u << 20), ro = mksrd(P.wo, 4u << 20);
    const unsigned wvo = 8 * g * 8192 + h * 256 + (32 * w + 2 * c) * 2;
    const unsigned woo = (h * 128 + 8 * g) * 1024 + (128 * w + 8 * c) * 2;
    const unsigned xo = OFF_XN + w * 8192 + lane * 16, l16 = lane * 16;
    const unsigned oo = (P.rolemask & 64) ? OOR : OFF_VW + 8 * w * 65536 + (h >> 3) * 16384 + (h & 7) * 1024 + lane * 16;
    const unsigned fo = lane == 0 ? C_VWF + (w * 32 + h) * 128 : OOR;
    int budget = SPIN_CAP;
    asm volatile(
        "s_mov_b32 s63, 0x05040100\ns_mov_b32 s64, 0x07060302\n"
        ".set wl_i, 0\n.rept 32\n"
        "s_mov_b32 s62, (32*(wl_i>>3) + (wl_i&4))*1024\n"
        "buffer_load_dwordx4 v[128+wl_i*4:131+wl_i*4], %[woo], %[ro], s62 offen offset:(wl_i&3)*1024\n"
        ".set wl_i, wl_i+1\n.endr\n"
        ".set wl_t, 0\n" VW_VISSUE
        ".set wl_p, 0\n.rept 2\n"
        "s_waitcnt vmcnt(32)\n" VW_VISSUE
        ".set wl_i, 0\n.rept 8\n.set wl_k, 0\n.rept 4\n"
        ".set wl_b, 128+wl_p*64+wl_i*8+wl_k\n.set wl_d, 128+wl_k*32+wl_p*8+wl_i\n"
        "v_perm_b32 v84, v[wl_b+4], v[wl_b], s63\nv_perm_b32 v85, v[wl_b+4], v[wl_b], s64\n"
        "v_accvgpr_write_b32 a[wl_d], v84\nv_accvgpr_write_b32 a[wl_d+16], v85\n"
        ".set wl_k, wl_k+1\n.endr\n.set wl_i, wl_i+1\n.endr\n"
        ".set wl_p, wl_p+1\n.endr\n"
        ".set wl_p, 0\n.rept 8\n"
        ".if wl_p < 6\ns_waitcnt vmcnt(32)\n.elseif wl_p == 6\ns_waitcnt vmcnt(16)\n.else\ns_waitcnt vmcnt(0)\n.endif\n"
        ".if wl_t < 8\n" VW_VISSUE ".endif\n"
        ".set wl_i, 0\n.rept 8\n"
        ".set wl_b, 96+wl_p*16+wl_i*2\n.set wl_d, wl_p*16+(wl_i>>2)*8+(wl_i&3)\n"
        "v_perm_b32 v84, v[wl_b+1], v[wl_b], s63\nv_perm_b32 v85, v[wl_b+1], v[wl_b], s64\n"
        "v_accvgpr_write_b32 a[wl_d], v84\nv_accvgpr_write_b32 a[wl_d+4], v85\n"
        ".set wl_i, wl_i+1\n.endr\n.set wl_p, wl_p+1\n.endr\n"
        CLRWAIT(0)
        "s_lshl_b32 s68, %[w], 13\n"
        "s_mov_b32 m0, s68\ns_mov_b32 s66, 0\n" XDMA8
        "s_add_u32 m0, s68, 0x8000\ns_mov_b32 s66, 0x8000\n" XDMA8
        "s_add_u32 s73, s68, 0x10000\ns_mov_b32 s74, s68\ns_add_u32 s75, s68, 0x8000\ns_mov_b32 s66, 0x10000\n"
        "s_mov_b32 s69, -1\ns_sub_u32 s71, s63, 0x200000\n"
        "s_lshl_b32 s67, %[w], 10\ns_add_u32 s67, s67, 0x18000\n"
        "v_mov_b32 v60, %[l16]\nv_add_u32 v62, s67, %[l16]\nv_add_u32 v63, 0x18000, %[l16]\n"
        "s_waitcnt vmcnt(8)\ns_barrier\n"
        "ds_read_b128 v[96:99], v60\nds_read_b128 v[100:103], v60 offset:1024\nds_read_b128 v[104:107], v60 offset:2048\n"
        "s_waitcnt lgkmcnt(0)\n"
        ".set vw_f, 1\n" VW_BODY
        "s_mov_b32 s70, 0x40000000\n"
        ".p2align 6\nLloop%=:\n"
        ".set vw_f, 0\n" VW_BODY
        "s_cmp_lt_u32 s69, 64\n"
        "s_cbranch_scc1 Lloop%=\n"
        "s_nop 7\n"
        ".set vw_i, 4\n.rept 4\n" VW_CVT(0, 1) VW_CVT(2, 3) VW_ST("s70") ".set vw_i, vw_i+1\n.endr\n"
        "v_mov_b32 v248, 64\n"
        "s_waitcnt vmcnt(0) lgkmcnt(0)\n"
        "buffer_store_dword v248, %[fo], %[srd], 0 offen sc1\n"
        : [bud] "+s"(budget)
        : [rv] "s"(rv), [ro] "s"(ro), [wvo] "v"(wvo), [woo] "v"(woo), [srd] "s"(srd), [xo] "v"(xo), [l16] "v"(l16), [oo] "v"(oo), [fo] "v"(fo), [w] "s"(w)
        : ACL, CL8(6), CL8(7), CL8(8), CL8(9), CL8(10), CL8(11), CL8(12), CL8(13), CL8(14), CL8(15), CL8(16), CL8(17), CL8(18), CL8(19), CL8(20), CL8(21),
          CL8(22), CL8(23), CL8(24), "v250", "v251", "v252", "v253", "v254", "v255", SCL, "vcc", "scc", "memory");
    if (budget < 0 && lane == 0) st32<SC1>(6, rws, C_ERR, 0);
    STAMP(224 + h);
}

struct TrueT { static constexpr bool value = true; };
struct FalseT { static constexpr bool value = false; };
DEV float bfr(float v) { return (float)(__bf16)v; }
DEV void role_ffn(const Params& P, rsrc_t rws, char* lds, int s, int w, int lane, u32x2 ep, int& budget) {
    const int g = lane >> 4, c = lane & 15;
    u32x2 tag;
    const rsrc_t rx = mkrs(P.x, 2048 * 512 * 2);
    {
        const int r8 = lane & 7, q = lane >> 3;
        u32x4 xv[8];
#pragma unroll
        for (int i = 0; i < 8; ++i) xv[i] = ld128<0>(rx, (32 * s + 8 * w + r8) * 1024 + q * 16 + i * 128, 0);
        f32x2 v[32];
#pragma unroll
        for (int i = 0; i < 8; ++i)
#pragma unroll
            for (int k = 0; k < 4; ++k) v[4 * i + k] = f32x2{asf(xv[i][k] << 16), asf(xv[i][k] & 0xFFFF0000u)};
        auto rowred = [](float a) { a += dppf<0x128>(a); a = add16(a); return fold32(a, a); };
        f32x2 sm = v[0];
#pragma unroll
        for (int k = 1; k < 32; ++k) sm += v[k];
        const float mean = rowred(sm.x + sm.y) * (1.0f / 512.0f);
        f32x2 sq = {0.f, 0.f};
#pragma unroll
        for (int k = 0; k < 32; ++k) { const f32x2 d = v[k] - mean; sq = __builtin_elementwise_fma(d, d, sq); }
        const float var = bfr(rowred(sq.x + sq.y) * (1.0f / 512.0f));
        const float mb = bfr(mean);
        const float den = bfr(sqrtf(bfr(var + 1e-5f)));
        const float rc = 1.0f / den;
        const unsigned dst = (w >> 1) * 16384 + (q >> 2) * 1024 + ((q & 3) * 16 + (w & 1) * 8 + r8) * 16;
#pragma unroll
        for (int i = 0; i < 8; ++i) {
            u32x4 o;
#pragma unroll
            for (int k = 0; k < 4; ++k) {
                const f32x2 d = v[4 * i + k] - mb;
                const unsigned db = pk(d.x, d.y);
                const f32x2 a = {asf(db << 16), asf(db & 0xFFFF0000u)};
                f32x2 qv = a * rc;
                const f32x2 rem = __builtin_elementwise_fma(-qv, f32x2{den, den}, a);
                qv = __builtin_elementwise_fma(rem, f32x2{rc, rc}, qv);
                o[k] = pk(qv.x, qv.y);
            }
            st128<SC1>(o, rws, OFF_XN + dst + i * 2048, s * 32768);
            *(u32x4*)(lds + dst + i * 2048) = o;
        }
        if (threadIdx.x < 32) st128<SC1>(u32x4{0, 0, 0, 0}, rws, C_CNT + s * 512 + threadIdx.x * 16, 0);
        tag = next_tag(ep);
        WAIT_VM0();
        BAR();
        if (threadIdx.x == 0) st64<SC1>(u32x2{tag.x | 0xffffu, tag.y}, rws, C_XF + s * 8, 0);
    }
    STAMP(960 + s);
    const rsrc_t r1 = mkrs(P.w1, (P.rolemask & 256) ? 0 : 512 * 1376 * 2), r2 = mkrs(P.w2, (P.rolemask & 256) ? 0 : 1376 * 512 * 2);
    const unsigned vo2 = 32 * g * 1024 + (128 * w + 8 * c) * 2;
    u32x4 raw[4][8];
    f32x4 acc[8][2];
    const int rot = s >> 2, rot2 = (rot * 11) >> 2;
    auto issue1 = [&](int u, int sl) {
        const unsigned col = (w + 4 * (u >> 4)) * 128 + 8 * c;
        const unsigned vo = col < 1376 ? 8 * g * 2752 + col * 2 : OOR;
#pragma unroll
        for (int j = 0; j < 8; ++j) raw[sl][j] = ld128<0>(r1, vo, (((u + rot) & 15) * 32 + j) * 2752);
    };
    auto issue2 = [&](int k0, int sl) {
        const int k = k0 < 44 ? (k0 + rot2 < 44 ? k0 + rot2 : k0 + rot2 - 44) : 44;
#pragma unroll
        for (int j = 0; j < 8; ++j) raw[sl][j] = ld128<0>(r2, vo2 + (j & 3) * 1024, ((k >> 2) * 128 + (k & 3) * 8 + (j & 4)) * 1024);
    };
    auto frags = [&](int sl, u32x4 (&F)[8]) {
#pragma unroll
        for (int jj = 0; jj < 4; ++jj)
#pragma unroll
            for (int i = 0; i < 4; ++i) { F[2 * i][jj] = plo(raw[sl][2 * jj][i], raw[sl][2 * jj + 1][i]); F[2 * i + 1][jj] = phi(raw[sl][2 * jj][i], raw[sl][2 * jj + 1][i]); }
    };
    auto zero = [&]() {
#pragma unroll
        for (int i = 0; i < 8; ++i) { acc[i][0] = f32x4{0, 0, 0, 0}; acc[i][1] = f32x4{0, 0, 0, 0}; }
    };
    const int nst = w < 3 ? 48 : 32;
#pragma unroll
    for (int i = 0; i < 4; ++i) issue1(i, i);
    zero();
    auto group = [&](int u, auto last) {
#pragma unroll
        for (int i = 0; i < 4; ++i) {
            SCHED();
            const u32x4 x0 = *(const u32x4*)(lds + ((u + i + rot) & 15) * 1024 + lane * 16), x1 = *(const u32x4*)(lds + 16384 + ((u + i + rot) & 15) * 1024 + lane * 16);
            u32x4 F[8];
            frags(i, F);
            if constexpr (decltype(last)::value) issue2(i, i); else issue1(u + 4 + i, i);
#pragma unroll
            for (int n = 0; n < 8; ++n) { acc[n][0] = mfma(F[n], x0, acc[n][0]); acc[n][1] = mfma(F[n], x1, acc[n][1]); }
        }
    };
    auto hidden = [&](int u) {
        char* hb = lds + 32768 + (w + 4 * (u >> 4)) * 8192 + lane * 16;
#pragma unroll
        for (int r = 0; r < 4; ++r)
#pragma unroll
            for (int t = 0; t < 2; ++t) {
                u32x4 d;
#pragma unroll
                for (int n = 0; n < 4; ++n) d[n] = pk(fmaxf(acc[2 * n][t][r], 0.f), fmaxf(acc[2 * n + 1][t][r], 0.f));
                *(u32x4*)(hb + r * 2048 + t * 1024) = d;
            }
        zero();
    };
#pragma nounroll
    for (int u = 0; u < nst - 4; u += 4) {
        group(u, FalseT());
        if ((u & 15) == 12) hidden(u);
    }
    group(nst - 4, TrueT());
    hidden(nst - 4);
    BAR();
    STAMP(832 + s);
#pragma nounroll
    for (int k = 0; k < 44; k += 4) {
#pragma unroll
        for (int i = 0; i < 4; ++i) {
            SCHED();
            const int kr = k + i + rot2 < 44 ? k + i + rot2 : k + i + rot2 - 44;
            const u32x4 h0 = *(const u32x4*)(lds + 32768 + kr * 2048 + lane * 16), h1 = *(const u32x4*)(lds + 33792 + kr * 2048 + lane * 16);
            u32x4 F[8];
            frags(i, F);
            issue2(k + 4 + i, i);
#pragma unroll
            for (int n = 0; n < 8; ++n) { acc[n][0] = mfma(h0, F[n], acc[n][0]); acc[n][1] = mfma(h1, F[n], acc[n][1]); }
        }
    }
#pragma unroll
    for (int t = 0; t < 2; ++t)
#pragma unroll
        for (int r = 0; r < 4; ++r) {
            const unsigned tok = 32 * s + 16 * t + 4 * g + r;
            const u32x4 xv = ld128<0>(rx, tok * 1024 + (128 * w + 8 * c) * 2, 0);
            u32x4 o0, o1;
#pragma unroll
            for (int i = 0; i < 2; ++i) {
                o0[2 * i] = asu(acc[2 * i][t][r] + asf(xv[i] << 16)); o0[2 * i + 1] = asu(acc[2 * i + 1][t][r] + asf(xv[i] & 0xFFFF0000u));
                o1[2 * i] = asu(acc[4 + 2 * i][t][r] + asf(xv[2 + i] << 16)); o1[2 * i + 1] = asu(acc[5 + 2 * i][t][r] + asf(xv[2 + i] & 0xFFFF0000u));
            }
            st128<SC1>(o0, rws, OFF_BASE + tok * 2048 + (128 * w + 8 * c) * 4, 0);
            st128<SC1>(o1, rws, OFF_BASE + tok * 2048 + (128 * w + 8 * c) * 4 + 16, 0);
        }
    wait_tags(rws, C_XF + lane * 8, tag, budget);
    WAIT_VM0();
    BAR();
    if (threadIdx.x == 0) st32<SC1>(1, rws, C_BF + s * 4, 0);
    STAMP(256 + s);
}

DEV void role_out(const Params& P, rsrc_t rws, char* lds, int id, int w, int lane, u32x2 ep, int& budget) {
    const int g = lane >> 4, c = lane & 15;
    const int set = id >> 4, tt = (id >> 3) & 1, cg = id & 7;
    const int j0 = 8 * (cg >> 1) + 4 * (cg & 1);
    const int col = 128 * (cg >> 1) + 8 * c + 4 * (cg & 1);
    unsigned seen = 0;
    const rsrc_t ro = mkrs(P.out, 2048 * 512 * 2);
    const u32x2 tag = next_tag(ep);
    const unsigned h2 = (RFL(wait_tags(rws, C_HM, tag, budget)) >> 10) * 8192;
#pragma nounroll
    for (;;) {
        const unsigned v = ld32<SC1>(rws, C_BF + lane * 4, 0);
        if (__all(v == 1u) || --budget < 0) break;
        __builtin_amdgcn_s_sleep(16);
    }
    asm volatile("" ::: "memory");
#pragma nounroll
    for (int s = set; s < NSEG; s += 4) {
        if (seen <= (unsigned)s) seen = wait_flags<8>(rws, C_VWF + ((cg >> 1) * 32 + w * 8) * 128, s, budget, lane);
        u32x4 VW[4][8];
#pragma unroll
        for (int i = 0; i < 4; ++i)
#pragma unroll
            for (int hh = 0; hh < 8; ++hh)
                VW[i][hh] = ld128<SC1>(rws, OFF_VW + (j0 + i) * 65536 + w * 16384 + (hh & 4) * 1024 + lane * 16 + (hh & 3) * 1024, s * 2 * MB + (h2 ^ ((~s & 1) << 13)));
        const unsigned tok = 32 * s + 16 * tt + 4 * g + w;
        const u32x4 bs = ld128<SC1>(rws, OFF_BASE + tok * 2048 + col * 4, 0);
        if (s >= 60) STAMP(704 + id);
        if (w == 0) {
            const unsigned need = s < 59 ? s + 2 : s + 1, fa = s < 59 ? C_U2F + (id & 31) * 128 : C_PF + (lane & 31) * 128;
#pragma nounroll
            for (;;) {
                const unsigned v = ld32<SC1>(rws, fa, 0);
                if (__all(v >= need) || --budget < 0) break;
                if (s >= 62 && __all(v >= min(62u, need))) __builtin_amdgcn_s_sleep(3);
                else __builtin_amdgcn_s_sleep(36);
            }
        }
        BAR();
        if (s >= 60) STAMP(768 + id);
        const unsigned uo = OFF_U + s * 524288 + (h2 ^ ((s & 4) << 11)) + j0 * 16384, us = 16384;
        u32x4 U[4][2];
#define ULD(i) U[i][0] = ld128<SC1>(rws, 2 * w * 1024 + lane * 16, uo + i * us); U[i][1] = ld128<SC1>(rws, 2 * w * 1024 + 1024 + lane * 16, uo + i * us);
        ULD(0) ULD(1) ULD(2) ULD(3)
        u32x4 Px[8], Pm[2];
#pragma unroll
        for (int hh = 0; hh < 8; ++hh) Px[hh] = ld128<SC1>(rws, OFF_P + tt * 40960 + (8 * w + (hh & 4)) * 1024 + lane * 16 + (hh & 3) * 1024, s * 81920);
#pragma unroll
        for (int n = 0; n < 2; ++n) Pm[n] = ld128<SC1>(rws, OFF_P + tt * 40960 + (32 + 2 * w + n) * 1024 + lane * 16, s * 81920);
        f32x4 acc[4];
#pragma unroll
        for (int i = 0; i < 4; ++i) acc[i] = f32x4{0, 0, 0, 0};
#pragma unroll
        for (int hh = 0; hh < 8; ++hh)
#pragma unroll
            for (int i = 0; i < 4; ++i) acc[i] = mfma(Px[hh], VW[i][hh], acc[i]);
        if (s >= 59) {
#pragma nounroll
            for (;;) {
                const unsigned m = max(max(max(umax4(U[0][0]), umax4(U[0][1])), max(umax4(U[1][0]), umax4(U[1][1]))),
                                       max(max(umax4(U[2][0]), umax4(U[2][1])), max(umax4(U[3][0]), umax4(U[3][1]))));
                if (__all(m != ~0u) || --budget < 0) break;
                ULD(0) ULD(1) ULD(2) ULD(3)
            }
        }
#pragma unroll
        for (int n = 0; n < 2; ++n)
#pragma unroll
            for (int i = 0; i < 4; ++i) acc[i] = mfma(Pm[n], U[i][n], acc[i]);
#pragma unroll
        for (int v = 0; v < 4; ++v) *(f32x4*)(lds + (w * 4 + v) * 1024 + lane * 16) = f32x4{acc[0][v], acc[1][v], acc[2][v], acc[3][v]};
        BAR();
        f32x4 t = __builtin_bit_cast(f32x4, bs);
#pragma unroll
        for (int o = 0; o < 4; ++o) t += *(const f32x4*)(lds + (o * 4 + w) * 1024 + lane * 16);
        st64<0>(u32x2{pk(t[0], t[1]), pk(t[2], t[3])}, ro, tok * 1024 + col * 2, 0);
    }
    if (id == 63 && threadIdx.x == 0) st64<SC1>(tag, rws, C_EPOCH, 0);
    if (budget < 0 && lane == 0) st32<SC1>(4, rws, C_ERR, 0);
    STAMP(640 + id);
}

DEV void role_cl(const Params& P, rsrc_t rws, u32x4 srd, char* lds, int id, int w, int lane) {
    const int tid = w * 64 + lane;
    int budget = SPIN_CAP;
    const u32x2 ep = ld64<SC1>(rws, C_EPOCH, 0);
    const u32x4 sent = {~0u, ~0u, ~0u, ~0u};
    u32x2 tag;
    unsigned hm, h2;
    if (P.rolemask & 2) {
    unsigned m0v;
    {
        const unsigned short* m0 = (const unsigned short*)P.mem0;
        const int r = (lane & 31) >> 2, c0 = 8 * (lane >> 5) + 2 * (lane & 3);
        const int col = 128 * (id >> 3) + 8 * c0 + (id & 7);
        m0v = nosent((unsigned)m0[r * 512 + col] | ((unsigned)m0[r * 512 + col + 8] << 16));
    }
    {
        int best[2] = {1 << 30, 1 << 30};
        const unsigned reg = ((4 * id + w) % 10) * 2 * MB + MB + ((4 * id + w) / 10) * 65536 + lane * 4;
#pragma nounroll
        for (int i = 0; i < 4; ++i) {
            const long long t0 = __builtin_amdgcn_s_memtime();
            st32<SC1>(~0u, rws, reg + (i & 1) * 8192 + (i >> 1) * 256, 0);
            WAIT_VM0();
            const int dt = (int)(__builtin_amdgcn_s_memtime() - t0);
            if (i & 1) best[1] = min(best[1], dt); else best[0] = min(best[0], dt);
        }
        if (lane == 0) *(unsigned*)(lds + w * 4) = best[0] < best[1] ? 0u : 1u << w;
    }
    BAR();
    tag = next_tag(ep);
    {
        const u32x4 a = *(const u32x4*)lds;
        if (tid == 0) st64<SC1>(u32x2{tag.x | a.x | a.y | a.z | a.w, tag.y}, rws, C_VOTE + id * 8, 0);
    }
    STAMP(64 + id);
    hm = close_halves(rws, tag, lane, budget);
    h2 = (hm >> 10) * 8192;
    if (id == 0 && tid == 0) st64<SC1>(u32x2{tag.x | hm, tag.y}, rws, C_HM, 0);
    if (w < 3) {
        st32<SC1>(m0v, rws, OFF_COMM + (hm & 1) * 8192 + w * 16384 + id * 256 + lane * 4, 0);
        st32<SC1>(m0v, rws, OFF_COMM + (hm & 1) * 8192 + w * 16384 + id * 256 + lane * 4, 49152);
    }
#pragma unroll
    for (int i = 0; i < 16; ++i) st128<SC1>(sent, rws, OFF_U + h2 + id * 16384 + tid * 16, (i >> 2) * 524288 + (i & 1) * 4096 + (i & 2) * 16 * MB);
#pragma unroll
    for (int i = 0; i < 6; ++i) reset_piece(rws, i, id, tid, hm);
    STAMP(96 + id);
    WAIT_VM0();
    BAR();
    if (tid == 0) st64<SC1>(u32x2{tag.x | hm, tag.y}, rws, C_MSG + id * 8, 0);
    STAMP(352 + id);
    }
    if (!(P.rolemask & 1)) return;
    tag = next_tag(ep);
    hm = RFL(wait_tags(rws, C_MSG + (lane & 31) * 8, tag, budget));
    h2 = (hm >> 10) * 8192;
    wait_tags(rws, C_XF + lane * 8, tag, budget);
    unsigned T = (unsigned)__builtin_amdgcn_s_memrealtime() - P.dly[11];
#pragma nounroll
    for (int i = 0; i < 60; ++i) {
        while ((int)(T + P.dly[11] - (unsigned)__builtin_amdgcn_s_memrealtime()) > 0) __builtin_amdgcn_s_sleep(1);
        const unsigned ts = OFF_TS + (i + 1) * 16384 + ((hm >> 9) & 1) * 8192 + id * 128;
        unsigned v;
        for (;;) {
            v = RFL(ld32<SC1>(rws, ts, 0));
            if (v != ~0u || --budget < 0) break;
            __builtin_amdgcn_s_sleep(12);
        }
        asm volatile("" ::: "memory");
        T += min((int)(v - T), 0x400);
        const unsigned ro = OFF_U + (i + 4) * 524288 + (h2 ^ (((i + 4) & 4) << 11)) + id * 16384 + tid * 16;
        st128<SC1>(sent, rws, ro, 0); st128<SC1>(sent, rws, ro, 4096); st128<SC1>(sent, rws, ro, 32 * MB); st128<SC1>(sent, rws, ro, 32 * MB + 4096);
        reset_piece(rws, i + 6, id, tid, hm);
        WAIT_VM0();
        BAR();
        if (tid == 0) st32<SC1>(i + 1, rws, C_U2F + id * 128, 0);
    }
    if (budget < 0 && lane == 0) st32<SC1>(7, rws, C_ERR, 0);
}

__global__ void __launch_bounds__(256, 1) mega_kernel(Params P) {
    extern __shared__ __attribute__((aligned(16))) char lds[];
    const rsrc_t rws = mkrs(P.ws, WS_END);
    const int lane = threadIdx.x & 63;
    const int w = __builtin_amdgcn_readfirstlane(threadIdx.x >> 6);
    const unsigned vb = blockIdx.x, xcd = vb & 7, slot = vb >> 3;
    const u32x4 srd = mksrd(P.ws, WS_END);
    if (xcd < 4) {
        const int id = xcd * 8 + (slot & 7);
        if (slot < 8) { if (P.rolemask & 1) role_qk(P, rws, srd, lds, id, w, lane); }
        else if (slot < 16) { if (P.rolemask & 1) role_vo(P, rws, srd, lds, id, w, lane); }
        else if (slot < 24) { if (P.rolemask & 1) role_ow(P, rws, srd, lds, id, w, lane); }
        else { if (P.rolemask & 3) role_cl(P, rws, srd, lds, id, w, lane); }
    } else {
        const int id = (xcd - 4) * 8 + (slot & 7);
        if (slot < 8) { if (P.rolemask & 4) role_kq(P, rws, srd, id, w, lane); }
        else if (slot < 16) { if (P.rolemask & 16) role_vw(P, rws, srd, id, w, lane); }
        else {
            int budget = SPIN_CAP;
            const u32x2 tag = ld64<SC1>(rws, C_EPOCH, 0);
            if (P.rolemask & 2) role_ffn(P, rws, lds, (slot - 16) * 4 + (xcd - 4), w, lane, tag, budget);
            if (P.rolemask & 8) role_out(P, rws, lds, (xcd - 4) * 16 + (slot - 16), w, lane, tag, budget);
        }
    }
}

extern "C" void kernel_launch(void* const* d_in, const int* in_sizes, int n_in, void* d_out, int out_size, void* d_ws, size_t ws_size,
                              hipStream_t stream) {
    Params P;
    P.x = d_in[0]; P.mem0 = d_in[1]; P.wq = d_in[2]; P.wk = d_in[3]; P.wv = d_in[4]; P.wo = d_in[5];
    P.w1 = d_in[8]; P.w2 = d_in[9];
    P.out = d_out;
    P.ws = (unsigned char*)(((uintptr_t)d_ws + (2 * MB - 1)) & ~(uintptr_t)(2 * MB - 1));
    P.rolemask = 31;
    P.dly[0] = DLY_QK_M; P.dly[1] = DLY_QK_K; P.dly[2] = DLY_VO_U; P.dly[3] = DLY_D1; P.dly[13] = DLY_D2; P.dly[14] = DLY_DB; P.dly[15] = DLY_E1; P.dly[16] = DLY_E2; P.dly[4] = DLY_VO_M; P.dly[5] = DLY_MP; P.dly[6] = DLY_PP; P.dly[7] = DLY_X; P.dly[8] = DLY_Q2; P.dly[9] = DLY_FFN; P.dly[10] = DLY_PRE; P.dly[11] = DLY_PRS; P.dly[12] = DLY_PACE;
    (void)hipFuncSetAttribute((const void*)mega_kernel, hipFuncAttributeMaxDynamicSharedMemorySize, LDS_BYTES);
    mega_kernel<<<256, 256, LDS_BYTES, stream>>>(P);
}
```

```cpp
#include <hip/hip_runtime.h>
#include <hip/hip_bf16.h>
#include <stdint.h>

typedef __bf16 bf16x2 __attribute__((ext_vector_type(2)));
typedef __bf16 bf16x8 __attribute__((ext_vector_type(8)));
typedef __bf16 bf16x16 __attribute__((ext_vector_type(16)));
typedef float f32x2 __attribute__((ext_vector_type(2)));
typedef float f32x4 __attribute__((ext_vector_type(4)));
typedef unsigned u32x2 __attribute__((ext_vector_type(2)));
typedef unsigned u32x4 __attribute__((ext_vector_type(4)));
typedef unsigned u32x8 __attribute__((ext_vector_type(8)));
typedef unsigned u32x32 __attribute__((ext_vector_type(32)));
typedef __amdgpu_buffer_rsrc_t rsrc_t;

#define DEV __device__ __forceinline__
#define SC1 16

constexpr int NSEG = 64;
constexpr unsigned MB = 1u << 20;
constexpr unsigned OFF_COMM = 0;
constexpr unsigned OFF_CTRL = 18 * MB;
constexpr unsigned OFF_TS = 19 * MB;
constexpr unsigned OFF_XN = 20 * MB;
constexpr unsigned OFF_P = 26 * MB;
constexpr unsigned OFF_U = 32 * MB;
constexpr unsigned OFF_KF = 32 * MB;
constexpr unsigned OFF_QF = 64 * MB;
constexpr unsigned OFF_VW = 96 * MB;
constexpr unsigned WS_END = 224 * MB;
#define C_EPOCH_ 0x1200000
#define TSH 7
#define CLS 8
#define NPROBE 6
#define C_MSG_ 0x12e3000
#define C_XF_ 0x12e0000
#define C_HM_ 0x1200100
#define C_OF_ 0x120f200
constexpr unsigned C_VOTE = 0x12e2000;
constexpr unsigned C_EPOCH = C_EPOCH_;
constexpr unsigned C_MSG = C_MSG_;
constexpr unsigned C_HM = C_HM_;
constexpr unsigned C_XF = C_XF_;
constexpr unsigned C_CNT = OFF_CTRL + 32768;
constexpr unsigned C_KQF = C_CNT;
constexpr unsigned C_VWF = C_CNT + 4096;
constexpr unsigned C_U2F = C_CNT + 20480;
constexpr unsigned C_PF = C_CNT + 24576;
constexpr unsigned C_BF = C_CNT + 28672;
constexpr unsigned C_ERR = C_CNT + 28672 + 256;
constexpr unsigned C_OF = C_CNT + 28672 + 512;
constexpr unsigned C_DBG = OFF_CTRL + 4096;
constexpr unsigned OOR = 0x40000000u;
static_assert(C_EPOCH == OFF_CTRL && C_OF == C_OF_, "");

constexpr int SPIN_CAP = 1 << 19;
constexpr int LDS_BYTES = 128 * 1024;

#define DLY_QK_M 180
#define DLY_QK_K 12
#define DLY_VO_U 102
#define DLY_D1 11
#define DLY_D2 20
#define DLY_DB 8
#define DLY_VO_M 115

#define DLY_MP 3
#define DLY_PP 80

#define DLY_X 38
#define DLY_Q2 18

#define DLY_PRE 38
#define DLY_PRS 80
#define DLY_PACE 9
#define DLY_FFN 8

#define DLY_E1 19
#define DLY_E2 38
#define DLY_VP 14
#define DLY_CL 32
#define DLY_S0 3
#define DLY_S1 3
#define DLY_S2 11
#define DLY_S3 3
#define OUTAUX 0
#define RKQ 15
#define RUB 7
#define RUA 63
#define RCM 15
#define RVW 31
#define LALIGN 4
#define KQE 12
#define XFAR 72
#define LEAD 5

struct Params {
    const void *x, *mem0, *wq, *wk, *wv, *wo, *w1, *w2;
    void* out;
    unsigned char* ws;
    unsigned rolemask;
    unsigned dly[23];
};

DEV rsrc_t mkrs(const void* p, unsigned bytes) { return __builtin_amdgcn_make_buffer_rsrc((void*)p, 0, (int)bytes, 0x00020000); }
DEV u32x4 mksrd(const void* p, unsigned bytes) { return u32x4{(unsigned)(uintptr_t)p, (unsigned)((uintptr_t)p >> 32) & 0xffffu, bytes, 0x00020000u}; }
template <int AUX> DEV u32x4 ld128(rsrc_t r, unsigned v, unsigned s) { return __builtin_amdgcn_raw_buffer_load_b128(r, (int)v, (int)s, AUX); }
template <int AUX> DEV u32x2 ld64(rsrc_t r, unsigned v, unsigned s) { return __builtin_amdgcn_raw_buffer_load_b64(r, (int)v, (int)s, AUX); }
template <int AUX> DEV unsigned ld32(rsrc_t r, unsigned v, unsigned s) { return __builtin_amdgcn_raw_buffer_load_b32(r, (int)v, (int)s, AUX); }
template <int AUX> DEV void st128(u32x4 d, rsrc_t r, unsigned v, unsigned s) { __builtin_amdgcn_raw_buffer_store_b128(d, r, (int)v, (int)s, AUX); }
template <int AUX> DEV void st64(u32x2 d, rsrc_t r, unsigned v, unsigned s) { __builtin_amdgcn_raw_buffer_store_b64(d, r, (int)v, (int)s, AUX); }
template <int AUX> DEV void st32(unsigned d, rsrc_t r, unsigned v, unsigned s) { __builtin_amdgcn_raw_buffer_store_b32(d, r, (int)v, (int)s, AUX); }

DEV unsigned pk(float lo, float hi) { f32x2 v = {lo, hi}; return __builtin_bit_cast(unsigned, __builtin_convertvector(v, bf16x2)); }
DEV unsigned plo(unsigned a, unsigned b) { return __builtin_amdgcn_perm(b, a, 0x05040100u); }
DEV unsigned phi(unsigned a, unsigned b) { return __builtin_amdgcn_perm(b, a, 0x07060302u); }
DEV float asf(unsigned u) { return __builtin_bit_cast(float, u); }
DEV unsigned asu(float f) { return __builtin_bit_cast(unsigned, f); }
DEV f32x4 mfma(u32x4 a, u32x4 b, f32x4 c) {
    return __builtin_amdgcn_mfma_f32_16x16x32_bf16(__builtin_bit_cast(bf16x8, a), __builtin_bit_cast(bf16x8, b), c, 0, 0, 0);
}
DEV f32x4 smf(u32x4 a, u32x8 b, f32x4 c, int idx) {
    return __builtin_amdgcn_smfmac_f32_16x16x64_bf16(__builtin_bit_cast(bf16x8, a), __builtin_bit_cast(bf16x16, b), c, idx, 0, 0);
}
DEV u32x8 cat(u32x4 a, u32x4 b) { return __builtin_shufflevector(a, b, 0, 1, 2, 3, 4, 5, 6, 7); }
DEV float fold32(float a, float b) { auto r = __builtin_amdgcn_permlane32_swap(asu(a), asu(b), false, false); return asf(r[0]) + asf(r[1]); }
DEV float max32(float a) { auto r = __builtin_amdgcn_permlane32_swap(asu(a), asu(a), false, false); return fmaxf(asf(r[0]), asf(r[1])); }
DEV float max16(float a) { auto r = __builtin_amdgcn_permlane16_swap(asu(a), asu(a), false, false); return fmaxf(asf(r[0]), asf(r[1])); }
DEV float add16(float a) { auto r = __builtin_amdgcn_permlane16_swap(asu(a), asu(a), false, false); return asf(r[0]) + asf(r[1]); }
template <int CTRL> DEV float dppf(float a) { return asf(__builtin_amdgcn_update_dpp(0u, asu(a), CTRL, 0xf, 0xf, true)); }
DEV float rowmax(float a) { a = fmaxf(a, dppf<0x128>(a)); a = fmaxf(a, dppf<0x124>(a)); a = fmaxf(a, dppf<0x122>(a)); return fmaxf(a, dppf<0x121>(a)); }
DEV float rowsum(float a) { a += dppf<0x128>(a); a += dppf<0x124>(a); a += dppf<0x122>(a); return a + dppf<0x121>(a); }
DEV unsigned umax4(u32x4 a) { return max(max(a.x, a.y), max(a.z, a.w)); }
DEV unsigned pofs(unsigned o) { return 16384 * (6 + (o >> 13)) + (o & 8191); }
DEV unsigned nosent(unsigned a) { return min(a, 0xFFFFFFFEu); }

#define PIN_A(x) asm volatile("" : "+a"(x))
#define SCHED() __builtin_amdgcn_sched_barrier(0)
#define RFL(x) __builtin_amdgcn_readfirstlane(x)
#define WAIT_VM0() asm volatile("s_waitcnt vmcnt(0)" ::: "memory")
#define FRESH() asm volatile("" ::: "memory")
#define BAR() do { asm volatile("s_waitcnt lgkmcnt(0)" ::: "memory"); __builtin_amdgcn_s_barrier(); asm volatile("" ::: "memory"); } while (0)

DEV void hold(unsigned t0, unsigned n) {
    while ((unsigned)__builtin_amdgcn_s_memrealtime() - t0 < n) __builtin_amdgcn_s_sleep(2);
    asm volatile("" ::: "memory");
}
DEV void delay(int n) {
    int t;
    asm volatile("s_mov_b32 %0, %1\n\t.p2align 6\n"
                 "1:\n\ts_sub_u32 %0, %0, 1\n\ts_cmp_gt_i32 %0, 0\n\ts_cbranch_scc1 1b" : "=&s"(t) : "s"(n) : "scc", "memory");
}

template <int NW> DEV unsigned wait_flags(rsrc_t rws, unsigned off, unsigned need, int& budget, int lane) {
    unsigned v;
    const unsigned a = off + (lane & (NW - 1)) * 128;
#pragma nounroll
    for (;;) {
        FRESH();
        v = ld32<SC1>(rws, a, 0);
        if (__all(v > need) || --budget < 0) break;
        __builtin_amdgcn_s_sleep(4);
    }
    asm volatile("" ::: "memory");
    unsigned m = __builtin_amdgcn_readlane(v, 0);
#pragma unroll
    for (int i = 1; i < NW; ++i) m = min(m, (unsigned)__builtin_amdgcn_readlane(v, i));
    return m;
}

DEV void glds16(const unsigned char* g, char* l) {
    __builtin_amdgcn_global_load_lds((const __attribute__((address_space(1))) void*)g, (__attribute__((address_space(3))) void*)l, 16, 0, 0);
}

#define STAMP(slot)

constexpr float SC_LOG2 = 0.08838834764831845f * 1.4426950408889634f;

#define WNC ""
#define WNF ""
#define WSZ (4u << 20)
#define SMF "v_smfmac_f32_16x16x64_bf16 "
#define DPPM " row_mask:0xf bank_mask:0xf\n"
#define Z4(a, b, c, d) "v_mov_b32 v" #a ", 0\nv_mov_b32 v" #b ", 0\nv_mov_b32 v" #c ", 0\nv_mov_b32 v" #d ", 0\n"
#define DELAY(n, L) "s_mov_b32 s60, %[" n "]\n.p2align " STR(LALIGN) "\n" L "%=:\ns_sub_u32 s60, s60, 1\ns_cmp_gt_i32 s60, 0\ns_cbranch_scc1 " L "%=\n"
#define PT(k)
#define PFLUSH
#define PDUMP(cu)
#define CL8(p) "v" #p "0", "v" #p "1", "v" #p "2", "v" #p "3", "v" #p "4", "v" #p "5", "v" #p "6", "v" #p "7", "v" #p "8", "v" #p "9"
#define AC8(p) "a" #p "0", "a" #p "1", "a" #p "2", "a" #p "3", "a" #p "4", "a" #p "5", "a" #p "6", "a" #p "7", "a" #p "8", "a" #p "9"
#define ACL AC8(), AC8(1), AC8(2), AC8(3), AC8(4), AC8(5), AC8(6), AC8(7), AC8(8), AC8(9), AC8(10), AC8(11), AC8(12), AC8(13), AC8(14), AC8(15), AC8(16), \
            AC8(17), AC8(18), AC8(19), AC8(20), AC8(21), AC8(22), AC8(23), AC8(24), "a250", "a251", "a252", "a253", "a254", "a255"
#define CLM "v56", "v57", "v58", "v59", CL8(6), CL8(7), CL8(8), "v90", "v91", "v92", "v93", "v94", "v95"
#define SCL "s60", "s61", "s62", "s63", "s64", "s65", "s66", "s67", "s68", "s69", "s70", "s71", "s72", "s73", "s74", "s75", "s76", "s77", "s78", "s79", \
            "s80", "s81", "s82", "s83", "s84", "s85", "s86", "s87", "s88", "s89", "s90", "s91", "s92", "s93", "s94", "s95"

#define Q4(A, b0, b1, b2, b3, X, Y) SMF "v[128:131], " A ", a[" b0 "], %[idx]\n" X SMF "v[132:135], " A ", a[" b1 "], %[idx]\n" \
                                    SMF "v[136:139], " A ", a[" b2 "], %[idx]\n" Y SMF "v[140:143], " A ", a[" b3 "], %[idx]\n"
#define QZERO Z4(128, 129, 130, 131) Z4(132, 133, 134, 135) Z4(136, 137, 138, 139) Z4(140, 141, 142, 143)
#define SZERO Z4(232, 233, 234, 235) Z4(236, 237, 238, 239) Z4(240, 241, 242, 243)
#define WLW3 ".if wl_p < 6\ns_waitcnt vmcnt(32)\n.elseif wl_p == 6\ns_waitcnt vmcnt(16)\n.else\ns_waitcnt vmcnt(0)\n.endif\n"
#define QK_WISSUE \
    "s_add_u32 s61, %[w], wl_t/2\ns_and_b32 s61, s61, 3\ns_lshl_b32 s61, s61, 20\n" \
    ".set wl_i, 0\n.rept 16\n" \
    "s_add_u32 s62, s61, ((wl_t&1)*4 + 2*(wl_i>>3) + 64*((wl_i>>1)&1) + 16*((wl_i>>2)&1) + 8*(wl_i&1))*8192\n" \
    "buffer_load_dwordx2 v[128+(wl_t&3)*32+wl_i*2:129+(wl_t&3)*32+wl_i*2], %[wvo], %[rw], s62 offen" WNC "\n" \
    ".set wl_i, wl_i+1\n.endr\n" ".set wl_t, wl_t+1\n"
#define QK_WLOAD \
    "s_mov_b32 s63, 0x05040100\ns_mov_b32 s64, 0x07060302\n" \
    ".set wl_t, 0\n.rept 3\n" QK_WISSUE ".endr\n" \
    ".set wl_p, 0\n.rept 8\n" \
    WLW3 \
    ".if wl_t < 8\n" QK_WISSUE ".endif\n" \
    ".set wl_i, 0\n.rept 8\n" \
    ".set wl_b, 128+(wl_p&3)*32+wl_i*4\n" \
    "v_perm_b32 v120, v[wl_b+2], v[wl_b], s63\nv_perm_b32 v121, v[wl_b+2], v[wl_b], s64\n" \
    "v_perm_b32 v122, v[wl_b+3], v[wl_b+1], s63\nv_perm_b32 v123, v[wl_b+3], v[wl_b+1], s64\n" \
    "v_accvgpr_write_b32 a[wl_p*32+wl_i], v120\nv_accvgpr_write_b32 a[wl_p*32+8+wl_i], v121\n" \
    "v_accvgpr_write_b32 a[wl_p*32+16+wl_i], v122\nv_accvgpr_write_b32 a[wl_p*32+24+wl_i], v123\n" \
    ".set wl_i, wl_i+1\n.endr\n.set wl_p, wl_p+1\n.endr\n"
#define AST(k)
#define ASTL(k, n)
#define ASTF
#define ASTP(k, p)
#define ASTOP(role)
#define STR2(x) #x
#define STR(x) STR2(x)
#define CLRL "buffer_load_dwordx2 v[cr+7:cr+8], v[cr+1], %[srd], s62 offen sc1\nbuffer_load_dwordx2 v[cr+5:cr+6], v[cr+2], %[srd], s61 offen sc1\n"
#define CLRA(msg, r) \
    ".set cr, " #r "\n" \
    "v_mbcnt_lo_u32_b32 v[cr+1], -1, 0\nv_mbcnt_hi_u32_b32 v[cr+1], -1, v[cr+1]\n" \
    "s_mov_b32 s60, " STR(C_EPOCH_) "\ns_mov_b32 s62, " STR(C_XF_) "\n" \
    ".if " #msg "\ns_mov_b32 s61, " STR(C_MSG_) "\n.else\ns_mov_b32 s61, " STR(C_HM_) "\n.endif\n" \
    "v_lshlrev_b32 v[cr+1], " STR(TSH) ", v[cr+1]\nv_and_b32 v[cr+2], ((32<<" STR(TSH) ")-1)*" #msg ", v[cr+1]\n" \
    "buffer_load_dwordx2 v[cr+3:cr+4], off, %[srd], s60 sc1\n" CLRL
#define CLRB(n) \
    "s_waitcnt vmcnt(" #n ")\n" \
    "s_branch Lclrc%=\n" \
    "Lclr%=:\n" CLRL \
    "s_waitcnt vmcnt(0)\n" \
    "Lclrc%=:\n" \
    "v_readfirstlane_b32 s64, v[cr+3]\nv_readfirstlane_b32 s65, v[cr+4]\n" \
    "s_add_u32 s64, s64, 0x10000\ns_addc_u32 s65, s65, 0\ns_or_b32 s64, s64, 0xffff\n" \
    "v_cmp_ne_u32 vcc, s64, v[cr+7]\n" \
    "v_cmp_ne_u32 s[66:67], s65, v[cr+8]\n" \
    "s_or_b64 vcc, vcc, s[66:67]\n" \
    "v_or_b32 v[cr], 0xffff, v[cr+5]\nv_cmp_ne_u32 s[66:67], s64, v[cr]\ns_or_b64 vcc, vcc, s[66:67]\nv_cmp_ne_u32 s[66:67], s65, v[cr+6]\ns_or_b64 vcc, vcc, s[66:67]\n" \
    "s_cmp_eq_u64 vcc, 0\n" \
    "s_cbranch_scc1 Lclrd%=\n" \
    "s_sub_u32 %[bud], %[bud], 1\n" \
    "s_sleep " STR(CLS) "\n" \
    "s_cmp_lt_i32 %[bud], 0\n" \
    "s_cbranch_scc0 Lclr%=\n" \
    "Lclrd%=:\n" \
    "v_readfirstlane_b32 s61, v[cr+5]\n" \
    "s_bitcmp1_b32 s61, 10\ns_cselect_b32 s63, 0x2000, 0\n" \
    "s_bitcmp1_b32 s61, 9\ns_cselect_b32 s62, 0x2000, 0\n"
#define CLRWAIT(msg) CLRA(msg, 247) CLRB(0)
#define BIGSO(d, st, sh, rs, mask) "s_lshl_b32 s60, " st ", 13-" #rs "\ns_and_b32 s60, s60, 0x2000\ns_xor_b32 s60, s60, s72\ns_and_b32 " d ", " st ", " STR(mask) "\ns_lshl_b32 " d ", " d ", " #sh "\ns_or_b32 " d ", " d ", s60\n"
#define CLRSO "s_mov_b32 %[hm], s61\nv_add_u32 %[tso], s62, %[tso]\n"
#define SETSO(d, st) "s_and_b32 " d ", " st ", " STR(RCM) "\ns_lshr_b32 s60, " d ", 3\ns_bitcmp1_b32 %[hm], s60\ns_cselect_b32 s60, 0x2000, 0\ns_lshl_b32 " d ", " d ", 18\ns_or_b32 " d ", " d ", s60\n"
#define UBSO(d, a) "s_and_b32 " d ", " a ", (" STR(RUB) "+1)*0x80000-1\ns_add_u32 " d ", " d ", 0x2000000\n"
#define WAITT(off, L) \
    "s_add_u32 s65, s79, " off "\n" \
    L "a%=:\n" \
    "s_memrealtime s[76:77]\n" \
    "s_waitcnt lgkmcnt(0)\n" \
    "s_sub_i32 s78, s65, s76\n" \
    "s_cmp_gt_i32 s78, 16\n" \
    "s_cbranch_scc1 " L "a%=\n" \
    "s_mul_i32 s60, s78, 3\n" \
    "s_ashr_i32 s60, s60, 2\n" \
    ".p2align " STR(LALIGN) "\n" L "b%=:\ns_sub_u32 s60, s60, 1\ns_cmp_gt_i32 s60, 0\ns_cbranch_scc1 " L "b%=\n"
#define GRID ".p2align 6\nLpoll%=:\n" WAITT("0", "Lw") "s_cmp_lt_i32 s78, -10\ns_cselect_b32 s79, s76, s79\n" PT(0)
#define TSLOAD "s_lshl_b32 s60, %[s], 14\nbuffer_load_dwordx2 v[250:251], %[tso], %[srd], s60 offen sc1\n"
#define TSLOAD4 "s_lshl_b32 s60, %[s], 14\nbuffer_load_dwordx4 v[224:227], %[tso], %[srd], s60 offen sc1\n"
#define MAXR(a, k) "v_max_i32_dpp v" #a ", v" #a ", v" #a " row_ror:" #k DPPM
#define MAXL(d, a) "v_readlane_b32 " d ", v" #a ", 0\nv_readlane_b32 s77, v" #a ", 16\ns_max_i32 " d ", " d ", s77\n"
#define CLAMP(d, lo, hi) "s_max_i32 " d ", " d ", " #lo "\ns_min_i32 " d ", " d ", " #hi "\n"
#define TSFOLLOW(n) \
    "s_waitcnt vmcnt(" #n ")\n" \
    "v_cmp_eq_u32 vcc, -1, v250\n" \
    "v_subrev_u32 v250, s79, v250\n" \
    MAXR(251, 8) "s_nop 0\n" MAXR(250, 8) MAXR(251, 4) "s_nop 0\n" MAXR(250, 4) MAXR(251, 2) "s_nop 0\n" MAXR(250, 2) MAXR(251, 1) "s_nop 0\n" MAXR(250, 1) \
    MAXL("s78", 251) CLAMP("s78", 64, 110) MAXL("s76", 250) \
    "s_min_i32 s76, s76, 0x400\n" \
    "s_add_u32 s76, s76, s78\n" \
    "s_cmp_eq_u64 vcc, 0\n" \
    "s_cselect_b32 s76, s76, %[gd]\n" \
    "s_add_u32 s79, s79, s76\n"
#define TSMAX(n) \
    "s_waitcnt vmcnt(" #n ")\n" \
    "v_cmp_eq_u32 vcc, -1, v224\n" \
    "v_subrev_u32 v224, s79, v224\n" \
    "v_max_i32 v226, v226, v227\n" \
    MAXR(225, 8) MAXR(224, 8) MAXR(226, 8) MAXR(225, 4) MAXR(224, 4) MAXR(226, 4) MAXR(225, 2) MAXR(224, 2) MAXR(226, 2) MAXR(225, 1) MAXR(224, 1) MAXR(226, 1) \
    MAXL("s66", 225) CLAMP("s66", 64, 110) MAXL("s76", 224) MAXL("s67", 226) \
    "s_min_i32 s76, s76, 0x400\n" \
    "s_cmp_lt_i32 s67, 0\ns_cselect_b32 s67, %[p0], s67\n" CLAMP("s67", 84, 140)
#define ESTUP(t, t0) \
    "s_sub_i32 s60, " t ", " t0 "\ns_lshl_b32 s60, s60, 3\ns_sub_i32 s60, s60, %[est]\n" \
    "s_min_i32 s60, s60, %[cl]\ns_sub_i32 s64, 0, %[cl]\ns_max_i32 s60, s60, s64\n" \
    "s_add_i32 s60, s60, 2\ns_ashr_i32 s60, s60, 2\n"
#define ESTPUB(r) \
    "s_add_u32 s60, %[s], 2\ns_lshl_b32 s60, s60, 14\ns_cmp_lt_u32 %[s], 61\ns_cselect_b32 s60, s60, 0x40000000\n" \
    "s_lshr_b32 s64, %[est], 3\ns_add_u32 s64, s64, %[vp]\nv_mov_b32 v" #r ", s64\n" \
    "buffer_store_dword v" #r ", %[ew], %[srd], s60 offen sc1\n"
#define ESTQ "s_waitcnt lgkmcnt(0)\n" ESTUP("s70", "s79") "s_cmp_eq_u32 s75, 0\ns_cselect_b32 s60, s60, 0\ns_add_i32 %[est], %[est], s60\n"
#define MLD(b, so) \
    "buffer_load_dwordx4 v[" #b ":" #b "+3], %[mo], %[srd], " so " offen sc1\n" \
    "buffer_load_dwordx4 v[" #b "+4:" #b "+7], %[mo], %[srd], " so " offen offset:1024 sc1\n"
#define MISSUE(b) \
    MLD(b, "%[so]") "s_mov_b32 s75, 0\ns_add_u32 s61, %[so], 0x4000\ns_add_u32 s62, %[so], 0x8000\n" \
    DELAY("i1", "Lda") MLD(56, "s61") DELAY("i2", "Ldb") MLD(64, "s62") "Lchk1%=:\n"
#define MV8(d, s) ".set wl_i, 0\n.rept 8\nv_mov_b32 v[" #d "+wl_i], v[" #s "+wl_i]\n.set wl_i, wl_i+1\n.endr\n"
#define MUSE(b, k, n, r) \
    "s_waitcnt vmcnt(" #n ")\n" \
    "v_max3_u32 v244, v[" #r "], v[" #r "+1], v[" #r "+2]\nv_max3_u32 v245, v[" #r "+3], v[" #r "+4], v[" #r "+5]\n" \
    "v_max3_u32 v244, v244, v[" #r "+6], v[" #r "+7]\nv_max_u32 v244, v244, v245\n" \
    "v_cmp_eq_u32 vcc, -1, v244\n" \
    "s_cbranch_vccnz Lre" #k "%=\n" \
    MV8(b, r) "s_mov_b32 s75, " #k "\ns_branch Lchk1%=\n" \
    "Lre" #k "%=:\n"
#define MRETRY(b) \
    "Lretry%=:\n" \
    MLD(72, "%[so]") \
    Z4(128, 129, 130, 131) Z4(132, 133, 134, 135) \
    MUSE(b, 1, 4, 56) MUSE(b, 2, 2, 64) \
    "Lspin%=:\n" \
    MLD(80, "s61") MUSE(b, 3, 2, 72) \
    MLD(72, "%[so]") MUSE(b, 4, 2, 80) \
    "s_sub_u32 %[bud], %[bud], 1\n" \
    "s_cmp_lt_i32 %[bud], 0\n" \
    "s_cbranch_scc0 Lspin%=\n" \
    "s_branch Lgo%=\n"
#define MREAD(r) \
    "s_waitcnt lgkmcnt(0)\n" \
    "s_barrier\n" \
    "ds_read_b128 v[" #r ":" #r "+3], %[ap1]\nds_read_b128 v[" #r "+4:" #r "+7], %[ap1] offset:1024\n" \
    "ds_read_b128 v[" #r "+8:" #r "+11], %[ap2]\nds_read_b128 v[" #r "+12:" #r "+15], %[ap2] offset:1024\n" \
    "ds_read_b128 v[" #r "+16:" #r "+19], %[ap3]\nds_read_b128 v[" #r "+20:" #r "+23], %[ap3] offset:1024\n"
#define KRD(i) "ds_read_b128 v[200+" #i "*4:203+" #i "*4], %[kr] offset:" #i "*1024\n"
#define QRD(i) "ds_read_b128 v[176+" #i "*4:179+" #i "*4], %[qr] offset:" #i "*1024\n"
#define KDMA(i) "buffer_load_dwordx4 %[kxo], %[srd], s73 offen offset:" #i "*1024 sc1 lds\n"
#define QK_HEAD(X5, X6, n6) \
    GRID MISSUE(144) \
    "s_waitcnt vmcnt(5)\n" \
    SMF "v[128:131], v[144:147], a[0:7], %[idx]\n" \
    "ds_write_b128 %[mo], v[144:147]\n" \
    "v_max3_u32 v244, v144, v145, v146\n" \
    SMF "v[132:135], v[144:147], a[8:15], %[idx]\n" \
    "s_waitcnt vmcnt(4)\n" \
    "ds_write_b128 %[mo], v[148:151] offset:1024\n" \
    "v_max3_u32 v245, v147, v148, v149\n" \
    "v_max3_u32 v244, v244, v150, v151\n" \
    "v_max_u32 v244, v244, v245\n" \
    "v_cmp_eq_u32 vcc, -1, v244\n" \
    "s_cbranch_vccnz Lretry%=\n" \
    "Lgo%=:\n" PT(1) \
    MREAD(152) \
    SMF "v[136:139], v[144:147], a[16:23], %[idx]\n" SMF "v[140:143], v[144:147], a[24:31], %[idx]\n" \
    Q4("v[148:151]", "32:39", "40:47", "48:55", "56:63", "", "") \
    "s_waitcnt lgkmcnt(5)\n" Q4("v[152:155]", "64:71", "72:79", "80:87", "88:95", KRD(0) KRD(1), "") \
    "s_waitcnt lgkmcnt(6)\n" Q4("v[156:159]", "96:103", "104:111", "112:119", "120:127", KRD(2) KRD(3), "") \
    "s_waitcnt lgkmcnt(7)\n" Q4("v[160:163]", "128:135", "136:143", "144:151", "152:159", KRD(4) KRD(5), "") \
    "s_waitcnt lgkmcnt(8)\n" Q4("v[164:167]", "160:167", "168:175", "176:183", "184:191", KRD(6) KRD(7), "") \
    "s_waitcnt lgkmcnt(9)\n" Q4("v[168:171]", "192:199", "200:207", "208:215", "216:223", X5, "") \
    "s_waitcnt lgkmcnt(" #n6 ")\n" Q4("v[172:175]", "224:231", "232:239", "240:247", "248:255", X6, "") \
    "s_nop 1\n" \
    "v_permlane32_swap_b32 v128, v129\nv_permlane32_swap_b32 v130, v131\n" \
    "v_add_f32 v128, v128, v129\nv_add_f32 v130, v130, v131\n" \
    "v_permlane32_swap_b32 v132, v133\nv_permlane32_swap_b32 v134, v135\n" \
    "v_add_f32 v132, v132, v133\nv_add_f32 v134, v134, v135\n" \
    "v_cvt_pk_bf16_f32 v244, v128, v132\nv_cvt_pk_bf16_f32 v246, v130, v134\n" \
    "v_permlane32_swap_b32 v136, v137\nv_permlane32_swap_b32 v138, v139\n" \
    "v_permlane32_swap_b32 v140, v141\nv_permlane32_swap_b32 v142, v143\n" \
    "v_add_f32 v136, v136, v137\nv_add_f32 v140, v140, v141\n" \
    "v_cvt_pk_bf16_f32 v245, v136, v140\n"
#define QK_MID \
    "v_add_f32 v138, v138, v139\nv_add_f32 v142, v142, v143\n" \
    "v_cvt_pk_bf16_f32 v247, v138, v142\n"
#define KQT
#define QK_TAIL(n) \
    PFLUSH TSFOLLOW(n) PT(4) KQT \
    "s_add_u32 %[s], %[s], 1\n" \
    SETSO("%[so]", "%[s]") \
    "s_branch Lpoll%=\n" \
    MRETRY(144) \
    "Lend%=:\ns_waitcnt vmcnt(0)\n"

#define KQFLOAD \
    "buffer_load_dwordx4 v[244:247], off, %[srd], %[fq] sc1\n" \
    "s_waitcnt vmcnt(0)\n" \
    "v_min3_u32 v244, v244, v245, v246\n" \
    "v_min_u32 v244, v244, v247\n" \
    "s_nop 0\n" \
    "v_readfirstlane_b32 %[seen], v244\n"
#define KQFPRE "s_add_u32 s74, %[s], " STR(LEAD) "\ns_min_u32 s74, s74, 64\ns_cmp_lt_u32 %[seen], s74\ns_cselect_b32 s74, %[fq], 0x40000000\nbuffer_load_dwordx4 v[252:255], off, %[srd], s74 sc1\n"
#define QK_NEXT \
    TSLOAD KQFPRE \
    "Lnext%=:\n" \
    "s_add_u32 s63, %[s], 1\n" \
    "s_cmp_gt_u32 %[seen], s63\n" \
    "s_cbranch_scc1 Lgot%=\n" \
    "Lflag%=:\n" \
    KQFLOAD \
    "s_cmp_gt_u32 %[seen], s63\n" \
    "s_cbranch_scc1 Lgot%=\n" \
    "s_sub_u32 %[bud], %[bud], 1\n" \
    "s_sleep 2\n" \
    "s_cmp_lt_i32 %[bud], 0\n" \
    "s_cbranch_scc0 Lflag%=\n" \
    "Lgot%=:\n" \
    BIGSO("s73", "s63", 19, 2, RKQ) \
    KDMA(0) KDMA(1) KDMA(2) KDMA(3) \
    "s_waitcnt vmcnt(4)\n" \
    "v_min3_u32 v252, v252, v253, v254\n" \
    "v_min_u32 v252, v252, v255\n" \
    "s_nop 0\n" \
    "v_readfirstlane_b32 s74, v252\n" \
    "s_max_u32 %[seen], %[seen], s74\n"
#define QK_INIT \
    "s_xor_b32 s72, s63, 0x2000\n" \
    "s_mov_b32 %[s], -1\ns_mov_b32 s79, %[t0]\ns_mov_b32 s75, 1\n" \
    "s_lshl_b32 s60, %[w], 12\ns_add_u32 m0, s60, 0x8000\n" \
    "v_mov_b32 v250, -1\nv_mov_b32 v252, 0\n" \
    "s_branch Lnext%=\n"
#define MF "v_mfma_f32_16x16x32_bf16 "

DEV u32x2 next_tag(u32x2 e) {
    asm volatile("" : "+v"(e));
    const unsigned long long t = (((unsigned long long)(unsigned)RFL(e.y) << 32) | (unsigned)RFL(e.x)) + 0x10000;
    return u32x2{(unsigned)t & 0xffff0000u, (unsigned)(t >> 32)};
}
DEV unsigned wait_tags(rsrc_t rws, unsigned off, u32x2 tag, int& budget) {
    u32x2 v;
#pragma nounroll
    for (;;) {
        FRESH();
        v = ld64<SC1>(rws, off, 0);
        if (__all((v.x & 0xffff0000u) == tag.x && v.y == tag.y) || --budget < 0) break;
        __builtin_amdgcn_s_sleep(4);
    }
    asm volatile("" ::: "memory");
    return v.x & 0xffffu;
}
constexpr unsigned vmask(int r, int w) { unsigned m = 0; for (int l = 0; l < 32; ++l) if ((4 * l + w) % 10 == r) m |= 1u << l; return m; }
DEV unsigned close_halves(rsrc_t rws, u32x2 tag, int lane, int& budget) {
    const unsigned v = wait_tags(rws, C_VOTE + ((lane & 31) << TSH), tag, budget);
    const unsigned b0 = (unsigned)__ballot(v & 1), b1 = (unsigned)__ballot(v & 2), b2 = (unsigned)__ballot(v & 4), b3 = (unsigned)__ballot(v & 8);
    int c[10], odd = 0;
#pragma unroll
    for (int r = 0; r < 10; ++r) {
        c[r] = __builtin_popcount(b0 & vmask(r, 0)) + __builtin_popcount(b1 & vmask(r, 1)) + __builtin_popcount(b2 & vmask(r, 2)) + __builtin_popcount(b3 & vmask(r, 3));
        odd += (r & 1) ? c[r] : (r < 8 ? 13 : 12) - c[r];
    }
    const unsigned g = odd < 64 ? 1u : 0u;
    unsigned hm = g << 10;
#pragma unroll
    for (int r = 0; r < 10; ++r) hm |= (((r ^ g) & 1) ? (c[r] > 1 ? 1u : 0u) : (c[r] > 10 ? 1u : 0u)) << r;
    return hm;
}
DEV void reset_piece(rsrc_t rws, int b, int id, int tid, unsigned hm) {
    const unsigned o = id < 27 ? OFF_COMM + (b & RCM) * 262144 + ((hm >> ((b & RCM) >> 3)) & 1) * 8192 + (id >> 1) * 16384 + (id & 1) * 4096 : OFF_TS + b * 16384 + ((hm >> 9) & 1) * 8192;
    st128<SC1>(u32x4{~0u, ~0u, ~0u, ~0u}, rws, (id > 27 || b > 63 || (b == 0 && id < 12)) ? OOR : o + tid * 16, 0);
}

DEV void role_qk(const Params& P, rsrc_t rws, u32x4 srd, char* lds, int h, int w, int lane) {
    const int g = lane >> 4, c = lane & 15;
    const u32x4 rw = mksrd(w < 2 ? P.wq : P.wk, WSZ);
    const unsigned wvo = ((g >> 1) + 32 * (g & 1)) * 8192 + h * 256 + (64 * (w & 1) + 4 * c) * 2;
    int budget = SPIN_CAP, s;
    const int idx = (c < 8) ? 0x4444 : 0xEEEE;
    const int rho0 = 4 * (g & 1) + (g >> 1);
    const unsigned mo = w * 2048 + lane * 16;
    const unsigned ap1 = ((2 * w + 2) & 7) * 1024 + lane * 16, ap2 = ((2 * w + 4) & 7) * 1024 + lane * 16, ap3 = ((2 * w + 6) & 7) * 1024 + lane * 16;
    unsigned so, hm;
    unsigned pf = 16384 + w * 2048;
    const unsigned t0 = (unsigned)__builtin_amdgcn_s_memrealtime() - P.dly[0];
    unsigned tso = lane < 32 ? OFF_TS + lane * 128 : OOR;
    const unsigned i1 = P.dly[3] * 3 / 4, i2 = (P.dly[13] - P.dly[3]) * 3 / 4;
    const unsigned kxo = (w < 2 ? OFF_KF : OFF_QF) + h * 16384 + (w & 1) * 4096 + lane * 16;
    const unsigned kr = 32768 + lane * 16;
    const unsigned fq = C_KQF + h * 128;
    unsigned seen = 0;
    pf -= 32;
    const unsigned kra0 = 10240 + g * 128 + (((c & 7) + 2 * (g >> 1)) & 7) * 16, kra1 = 10240 + 512 + g * 128 + (((c & 7) + 1 + 2 * (g >> 1)) & 7) * 16;
    if (w < 2) {
        const int ga = 2 * ((c >> 1) & 1) + (c >> 3), i = 2 * ((c >> 2) & 1) + (c & 1);
        const unsigned wa0 = 8192 + w * 1024 + ga * 256 + ((rho0 + 2 * (ga & 1)) & 7) * 16 + i * 4;
        const unsigned wa1 = 8192 + w * 1024 + ga * 256 + ((rho0 + 2 + 2 * (ga & 1)) & 7) * 16 + i * 4;
        const int rq = 4 * w + 2 * ((c >> 2) & 1) + (c & 1);
        const unsigned qra = 8192 + g * 256 + (c >> 3) * 128 + ((rq + 2 * (g & 1)) & 7) * 16;
        const int r = 4 * w + 2 * (g & 1) + (g >> 1);
        unsigned pox = h * 640 + (c >> 3) * 256 + (c & 1) * 128 + r * 16 + ((c >> 1) & 3) * 4;
        unsigned pom = h * 640 + 512 + r * 16 + ((c & 7) >> 1) * 4;
        const unsigned pbx = pofs(pox + 20480), pbm = pofs(pom + 20480), pcx = pofs(pox + 40960), pcm = pofs(pom + 40960);
        pox = pofs(pox); pom = pofs(pom);
        unsigned est = (P.dly[2] - P.dly[17]) * 8 + 4, ew = (w == 1 && lane == 0) ? OFF_TS + h * 128 + 8 : OOR;
        asm volatile(
            QK_WLOAD AST(0) CLRWAIT(1) AST(1) ASTF CLRSO "v_add_u32 %[ew], s62, %[ew]\n"
            QK_INIT
            QK_HEAD("", "", 8)
            "ds_write2_b32 %[wa0], v244, v245 offset1:32\n"
            QK_MID
            "ds_write2_b32 %[wa1], v246, v247 offset1:32\n"
            "s_cmp_eq_u32 %[s], 63\n"
            "s_waitcnt lgkmcnt(0)\n"
            "s_barrier\n" PT(2)
            "s_cbranch_scc1 Lend%=\n"
            "ds_read_b128 v[176:179], %[qra]\nds_read_b128 v[180:183], %[qra] offset:1024\n"
            "ds_read_b128 v[184:187], %[kra0]\nds_read_b128 v[188:191], %[kra1]\n"
            "ds_read_b128 v[192:195], %[kra0] offset:1024\nds_read_b128 v[196:199], %[kra1] offset:1024\n"
            "s_waitcnt lgkmcnt(5)\n"
            SMF "v[232:235], v[176:179], v[200:207], %[idx]\n"
            SMF "v[236:239], v[176:179], v[216:223], %[idx]\n"
            "s_waitcnt lgkmcnt(4)\n"
            SMF "v[232:235], v[180:183], v[208:215], %[idx]\n"
            SMF "v[236:239], v[180:183], v[224:231], %[idx]\n"
            "s_waitcnt lgkmcnt(2)\n"
            SMF "v[240:243], v[176:179], v[184:191], %[idx]\n"
            "s_waitcnt lgkmcnt(0)\n"
            SMF "v[240:243], v[180:183], v[192:199], %[idx]\n"
            "v_permlane32_swap_b32 v232, v233\n"
            "v_add_f32 v232, v232, v233\n"
            "v_permlane32_swap_b32 v236, v237\n"
            "v_add_f32 v233, v236, v237\n"
            "v_max_f32 v244, v232, v233\n"
            "s_nop 1\n"
            "v_max_f32_dpp v244, v244, v244 row_ror:8" DPPM
            "v_permlane32_swap_b32 v240, v241\n"
            "v_add_f32 v240, v240, v241\n"
            "v_max_f32_dpp v244, v244, v244 row_ror:4" DPPM
            "s_nop 0\n"
            "v_max_f32_dpp v245, v240, v240 row_ror:4" DPPM
            "v_max_f32_dpp v244, v244, v244 row_ror:2" DPPM
            "s_nop 0\n"
            "v_max_f32_dpp v245, v245, v245 row_ror:2" DPPM
            "v_max_f32_dpp v244, v244, v244 row_ror:1" DPPM
            "s_nop 0\n"
            "v_max_f32_dpp v245, v245, v245 row_ror:1" DPPM
            "v_max_f32 v244, v244, v245\n"
            "v_mul_f32 v244, %[sc], v244\n"
            "v_fma_f32 v232, v232, %[sc], -v244\n"
            "v_fma_f32 v233, v233, %[sc], -v244\n"
            "v_exp_f32 v232, v232\n"
            "v_fma_f32 v240, v240, %[sc], -v244\n"
            "v_exp_f32 v233, v233\n"
            "v_exp_f32 v240, v240\n"
            "v_add_f32 v246, v232, v233\n"
            "v_fmac_f32 v246, 0.5, v240\n"
            "s_nop 0\n"
            "v_mov_b32_dpp v247, v240 quad_perm:[1,0,3,2]" DPPM
            "v_add_f32_dpp v246, v246, v246 row_ror:8" DPPM
            "v_cndmask_b32 v248, v240, v247, %[odd]\n"
            "v_cndmask_b32 v249, v247, v240, %[odd]\n"
            "v_add_f32_dpp v246, v246, v246 row_ror:4" DPPM
            "s_nop 1\n"
            "v_add_f32_dpp v246, v246, v246 row_ror:2" DPPM
            "s_nop 1\n"
            "v_add_f32_dpp v246, v246, v246 row_ror:1" DPPM
            "v_rcp_f32 v246, v246\n"
            "s_nop 0\n"
            "v_pk_mul_f32 v[232:233], v[232:233], v[246:247] op_sel_hi:[1,0] clamp\n"
            "v_cvt_pk_bf16_f32 v232, v232, v233\n"
            "buffer_store_dword v232, %[pox], %[srd], %[so] offen sc1\n"
            "v_pk_mul_f32 v[248:249], v[248:249], v[246:247] op_sel_hi:[1,0] clamp\n"
            "v_cvt_pk_bf16_f32 v248, v248, v249\n"
            "buffer_store_dword v248, %[pom], %[srd], %[so] offen sc1\n" PT(3)
            "s_memrealtime s[70:71]\n"
            "buffer_store_dword v232, %[pbx], %[srd], %[so] offen sc1\n"
            "buffer_store_dword v248, %[pbm], %[srd], %[so] offen sc1\n"
            "buffer_store_dword v232, %[pcx], %[srd], %[so] offen sc1\n"
            "buffer_store_dword v248, %[pcm], %[srd], %[so] offen sc1\n"
            ESTPUB(249)
            QK_NEXT
            QZERO SZERO ESTQ
            QK_TAIL(4)
            : [s] "=&s"(s), [so] "=&s"(so), [hm] "=&s"(hm), [bud] "+s"(budget), [seen] "+s"(seen), [pf] "+v"(pf), [tso] "+v"(tso), [est] "+s"(est), [ew] "+v"(ew)
            : [rw] "s"(rw), [wvo] "v"(wvo), [srd] "s"(srd), [mo] "v"(mo), [idx] "v"(idx), [ap1] "v"(ap1), [ap2] "v"(ap2), [ap3] "v"(ap3), [wa0] "v"(wa0), [wa1] "v"(wa1),
              [qra] "v"(qra), [kra0] "v"(kra0), [kra1] "v"(kra1), [pox] "v"(pox), [pom] "v"(pom), [pbx] "v"(pbx), [pbm] "v"(pbm), [pcx] "v"(pcx), [pcm] "v"(pcm), [kr] "v"(kr), [kxo] "v"(kxo), [i1] "s"(i1), [i2] "s"(i2),
              [gd] "s"(P.dly[0]), [cl] "s"(P.dly[18]), [vp] "s"(P.dly[17]), [t0] "s"(t0) ASTOP(0), [fq] "s"(fq), [sc] "s"(SC_LOG2), [odd] "s"(0xAAAAAAAAAAAAAAAAull), [w] "s"(w)
            : ACL, CLM, CL8(12), CL8(13), CL8(14), CL8(15), CL8(16), CL8(17), CL8(18), CL8(19), CL8(20), CL8(21), CL8(22), CL8(23), CL8(24),
              "v250", "v251", "v252", "v253", "v254", "v255", SCL, "vcc", "scc", "memory");
    } else {
        const int rot = ((c >> 1) & 1) + 2 * (c >> 3);
        const unsigned wa0 = 10240 + (w - 2) * 1024 + ((c >> 1) & 1) * 512 + (c >> 2) * 128 + ((rho0 + rot) & 7) * 16 + (c & 1) * 8;
        const unsigned wa1 = 10240 + (w - 2) * 1024 + ((c >> 1) & 1) * 512 + (c >> 2) * 128 + ((rho0 + 2 + rot) & 7) * 16 + (c & 1) * 8;
        const int tt = w - 2, tq = 16 * tt + c - 4 * g;
        u32x8 cm;
#pragma unroll
        for (int r = 0; r < 4; ++r) { cm[r] = r <= tq ? 0u : asu(-1.0e30f); cm[4 + r] = 16 + r <= tq ? 0u : asu(-1.0e30f); }
        const unsigned qr = 32768 + 8192 + tt * 4096 + lane * 16;
        const unsigned pxo = OFF_P + tt * 40960 + h * 1024 + lane * 16;
        const unsigned pmo = OFF_P + tt * 40960 + (32 + (h >> 2)) * 1024 + ((h & 3) * 16 + c) * 16 + (g & 1) * 8;
        const unsigned fk = (w == 2 && lane == 0) ? C_PF + h * 128 : OOR;
        asm volatile(
            QK_WLOAD AST(0) CLRWAIT(1) AST(1) ASTF CLRSO
            QK_INIT
            QK_HEAD(QRD(0) QRD(1), QRD(2) QRD(3), 10)
            "ds_write_b64 %[wa0], v[244:245]\n"
            QK_MID
            "ds_write_b64 %[wa1], v[246:247]\n"
            "s_waitcnt lgkmcnt(0)\n"
            "s_barrier\n" PT(2)
            MF "v[232:235], v[200:203], v[176:179], v[112:115]\n"
            MF "v[236:239], v[216:219], v[176:179], v[116:119]\n"
            MF "v[232:235], v[204:207], v[180:183], v[232:235]\n"
            MF "v[236:239], v[220:223], v[180:183], v[236:239]\n"
            MF "v[232:235], v[208:211], v[184:187], v[232:235]\n"
            MF "v[236:239], v[224:227], v[184:187], v[236:239]\n"
            "ds_read_b128 v[96:99], %[kra0]\nds_read_b128 v[100:103], %[kra1]\n"
            "ds_read_b128 v[104:107], %[kra0] offset:1024\nds_read_b128 v[108:111], %[kra1] offset:1024\n"
            MF "v[232:235], v[212:215], v[188:191], v[232:235]\n"
            MF "v[236:239], v[228:231], v[188:191], v[236:239]\n"
            "s_mul_i32 s61, %[s], 81920\n"
            "s_waitcnt lgkmcnt(0)\n"
            MF "v[240:243], v[96:99], v[176:179], 0\n"
            MF "v[240:243], v[100:103], v[180:183], v[240:243]\n"
            MF "v[240:243], v[104:107], v[184:187], v[240:243]\n"
            MF "v[240:243], v[108:111], v[188:191], v[240:243]\n"
            "v_max3_f32 v244, v232, v233, v234\n"
            "v_max3_f32 v245, v235, v236, v237\n"
            "v_max3_f32 v244, v244, v238, v239\n"
            "v_max_f32 v244, v244, v245\n"
            "s_nop 4\n"
            "v_max3_f32 v245, v240, v241, v242\n"
            "v_max3_f32 v244, v244, v245, v243\n"
            "v_mov_b32 v245, v244\n"
            "s_nop 1\n"
            "v_permlane16_swap_b32 v244, v245\n"
            "v_max_f32 v244, v244, v245\n"
            "v_mov_b32 v245, v244\n"
            "s_nop 1\n"
            "v_permlane32_swap_b32 v244, v245\n"
            "v_max_f32 v244, v244, v245\n"
            "v_mul_f32 v244, %[sc], v244\n"
            ".set wl_i, 232\n.rept 12\nv_fma_f32 v[wl_i], v[wl_i], %[sc], -v244\n.set wl_i, wl_i+1\n.endr\n"
            ".set wl_i, 232\n.rept 12\nv_exp_f32 v[wl_i], v[wl_i]\n.set wl_i, wl_i+1\n.endr\n"
            "v_pk_add_f32 v[246:247], v[232:233], v[234:235]\n"
            "v_pk_add_f32 v[248:249], v[236:237], v[238:239]\n"
            "v_pk_add_f32 v[244:245], v[240:241], v[242:243]\n"
            "v_pk_add_f32 v[246:247], v[246:247], v[248:249]\n"
            "v_add_f32 v244, v244, v245\n"
            "v_add_f32 v246, v246, v247\n"
            "v_fmac_f32 v246, 0.5, v244\n"
            "v_mov_b32 v247, v246\n"
            "s_nop 1\n"
            "v_permlane16_swap_b32 v246, v247\n"
            "v_add_f32 v246, v246, v247\n"
            "v_mov_b32 v247, v246\n"
            "s_nop 1\n"
            "v_permlane32_swap_b32 v246, v247\n"
            "v_add_f32 v246, v246, v247\n"
            "v_rcp_f32 v246, v246\n"
            "v_mov_b32 v244, %[s]\n"
            ".set wl_i, 232\n.rept 6\nv_pk_mul_f32 v[wl_i:wl_i+1], v[wl_i:wl_i+1], v[246:247] op_sel_hi:[1,0]\n.set wl_i, wl_i+2\n.endr\n"
            "v_cvt_pk_bf16_f32 v192, v232, v236\nv_cvt_pk_bf16_f32 v193, v233, v237\n"
            "v_cvt_pk_bf16_f32 v194, v234, v238\nv_cvt_pk_bf16_f32 v195, v235, v239\n"
            "v_cvt_pk_bf16_f32 v196, v240, v241\nv_cvt_pk_bf16_f32 v197, v242, v243\n"
            "buffer_store_dwordx4 v[192:195], %[pxo], %[srd], s61 offen sc1\n"
            "buffer_store_dwordx2 v[196:197], %[pmo], %[srd], s61 offen sc1\n"
            "s_cmp_eq_u32 %[s], 63\n"
            "s_cbranch_scc1 Lend%=\n"
            "s_cmp_ge_u32 %[s], 60\n"
            "s_cselect_b32 s61, 0, 0x40000000\n"
            "buffer_store_dword v244, %[fk], %[srd], s61 offen sc1\n" PT(3)
            QK_NEXT
            QZERO
            QK_TAIL(4)
            : [s] "=&s"(s), [so] "=&s"(so), [hm] "=&s"(hm), [bud] "+s"(budget), [seen] "+s"(seen), [pf] "+v"(pf), [tso] "+v"(tso)
            : [rw] "s"(rw), [wvo] "v"(wvo), [srd] "s"(srd), [mo] "v"(mo), [idx] "v"(idx), [ap1] "v"(ap1), [ap2] "v"(ap2), [ap3] "v"(ap3), [wa0] "v"(wa0), [wa1] "v"(wa1),
              [kra0] "v"(kra0), [kra1] "v"(kra1), [kxo] "v"(kxo), [pxo] "v"(pxo), [pmo] "v"(pmo), [kr] "v"(kr), [qr] "v"(qr), "{v[112:119]}"(cm), [i1] "s"(i1), [i2] "s"(i2),
              [gd] "s"(P.dly[0]), [t0] "s"(t0) ASTOP(0), [fq] "s"(fq), [fk] "v"(fk), [sc] "s"(SC_LOG2), [w] "s"(w)
            : ACL, CLM, "v96", "v97", "v98", "v99", CL8(10), "v110", "v111", CL8(12), CL8(13), CL8(14), CL8(15), CL8(16), CL8(17), CL8(18), CL8(19), CL8(20), CL8(21),
              CL8(22), CL8(23), CL8(24), "v250", "v251", "v252", "v253", "v254", "v255", SCL, "vcc", "scc", "memory");
    }
    WAIT_VM0();
    BAR();
    if (threadIdx.x == 0) st32<SC1>(NSEG, rws, C_PF + h * 128, 0);
    if (budget < 0 && lane == 0) st32<SC1>(1, rws, C_ERR, 0);
    STAMP(128 + h);
    PDUMP(h);
}

#define V2(A, b0, b1) SMF "v[128:131], " A ", a[" b0 "], %[idx]\n" SMF "v[132:135], " A ", a[" b1 "], %[idx]\n"
#define VWL(r, o, so) "buffer_load_dwordx4 v[" r "], %[vwo], %[srd], " so " offen offset:" o " sc1\n"
#define UM(d, A, b) SMF "v[" d "], " A ", a[" b "], %[idx]\n"
#define F0 "v[168:171]"
#define F1 "v[172:175]"
#define SW(a, b) "v_permlane32_swap_b32 v" #a ", v" #b "\n"
#define S16(a, b) "v_permlane16_swap_b32 v" #a ", v" #b "\n"
#define AD(a, b) "v_add_f32 v" #a ", v" #a ", v" #b "\n"
#define CV(d, a, b) "v_cvt_pk_bf16_f32 v" #d ", v" #a ", v" #b "\n"
#define MN(d) "v_min_u32 v" #d ", -2, v" #d "\n"
#define VZERO Z4(128, 129, 130, 131) Z4(132, 133, 134, 135)
#define UZERO Z4(176, 177, 178, 179) Z4(180, 181, 182, 183) Z4(184, 185, 186, 187) Z4(188, 189, 190, 191) \
              Z4(192, 193, 194, 195) Z4(196, 197, 198, 199) Z4(200, 201, 202, 203) Z4(204, 205, 206, 207)
#define VWFMIN(r, d) \
    "v_min_u32_dpp v" #r ", v" #r ", v" #r " row_ror:4" DPPM "s_nop 1\n" \
    "v_min_u32_dpp v" #r ", v" #r ", v" #r " row_ror:2" DPPM "s_nop 1\n" \
    "v_min_u32_dpp v" #r ", v" #r ", v" #r " row_ror:1" DPPM "s_nop 0\n" \
    "v_readfirstlane_b32 " d ", v" #r "\n"
#define VO_FLAG(L) \
    "Lflag" L "%=:\n" \
    "buffer_load_dword v248, %[fvo], %[srd], 0 offen sc1\n" \
    "s_waitcnt vmcnt(0)\n" \
    VWFMIN(248, "%[seen]") \
    "s_cmp_gt_u32 %[seen], s69\n" \
    "s_cbranch_scc1 Lhave" L "%=\n" \
    "s_sub_u32 %[bud], %[bud], 1\n" \
    "s_cmp_lt_i32 %[bud], 0\n" \
    "s_cbranch_scc1 Lhave" L "%=\n" \
    "s_sleep 2\n" \
    "s_branch Lflag" L "%=\n" \
    "Lhave" L "%=:\n"
#define VO_VISSUE \
    "s_add_u32 s61, s59, wl_t/2\ns_and_b32 s61, s61, 3\ns_lshl_b32 s61, s61, 20\n" \
    ".if wl_t < 2\n.set wl_d, 96+wl_t*16\n.else\n.set wl_d, 96+wl_t*16\n.endif\n" \
    ".set wl_i, 0\n.rept 16\n" \
    "s_add_u32 s62, s61, ((wl_t&1)*4 + 2*(wl_i>>3) + 64*((wl_i>>1)&1) + 16*((wl_i>>2)&1) + 8*(wl_i&1))*8192\n" \
    "buffer_load_dword v[wl_d+wl_i], %[wvo], %[rv], s62 offen" WNC "\n" \
    ".set wl_i, wl_i+1\n.endr\n" ".set wl_t, wl_t+1\n"
#define VO_WLOAD \
    "s_mov_b32 s63, 0x05040100\ns_mov_b32 s64, 0x07060302\n" \
    ".set wl_i, 0\n.rept 32\n" \
    "s_mov_b32 s62, (64*(wl_i>>4) + 32*((wl_i>>3)&1) + 8*((wl_i>>1)&1) + 2*((wl_i>>2)&1))*1024\n" \
    "buffer_load_dwordx4 v[128+wl_i*4:131+wl_i*4], %[woo], %[ro], s62 offen offset:(wl_i&1)*1024" WNC "\n" \
    ".set wl_i, wl_i+1\n.endr\n" \
    ".set wl_t, 0\n" VO_VISSUE \
    ".set wl_p, 0\n.rept 2\n" \
    "s_waitcnt vmcnt(32)\n" VO_VISSUE \
    ".set wl_i, 0\n.rept 8\n.set wl_k, 0\n.rept 4\n" \
    ".set wl_b, 128+wl_p*64+wl_i*8+wl_k\n" \
    "v_perm_b32 v84, v[wl_b+4], v[wl_b], s63\nv_perm_b32 v85, v[wl_b+4], v[wl_b], s64\n" \
    "v_accvgpr_write_b32 a[128+wl_k*32+wl_p*8+wl_i], v84\nv_accvgpr_write_b32 a[128+wl_k*32+16+wl_p*8+wl_i], v85\n" \
    ".set wl_k, wl_k+1\n.endr\n.set wl_i, wl_i+1\n.endr\n" \
    ".set wl_p, wl_p+1\n.endr\n" \
    ".set wl_p, 0\n.rept 8\n" \
    WLW3 \
    ".if wl_t < 8\n" VO_VISSUE ".endif\n" \
    ".set wl_i, 0\n.rept 8\n" \
    ".set wl_b, 96+wl_p*16+wl_i*2\n" \
    "v_perm_b32 v84, v[wl_b+1], v[wl_b], s63\nv_perm_b32 v85, v[wl_b+1], v[wl_b], s64\n" \
    "v_accvgpr_write_b32 a[wl_p*16+wl_i], v84\nv_accvgpr_write_b32 a[wl_p*16+8+wl_i], v85\n" \
    ".set wl_i, wl_i+1\n.endr\n.set wl_p, wl_p+1\n.endr\n"
DEV void role_vo(const Params& P, rsrc_t rws, u32x4 srd, char* lds, int h, int w, int lane) {
    const int g = lane >> 4, c = lane & 15;
    const u32x4 rv = mksrd(P.wv, WSZ), ro = mksrd(P.wo, WSZ);
    const unsigned wvo = ((g >> 1) + 32 * (g & 1)) * 8192 + h * 256 + (32 * w + 2 * c) * 2;
    const unsigned woo = (h * 128 + 16 * (g >> 1) + 4 * (g & 1)) * 1024 + (128 * w + 8 * c) * 2;
    int budget = SPIN_CAP, s;
    const int idx = (c < 8) ? 0x4444 : 0xEEEE;
    const int rho0 = 4 * (g & 1) + (g >> 1);
    const int vga = 2 * (w & 1) + (c >> 3);
    const unsigned va0 = 8192 + (w >> 1) * 1024 + (vga * 16 + ((c >> 2) & 1) * 8 + ((rho0 + 2 * (vga & 1)) & 7)) * 16 + (c & 3) * 4;
    const unsigned va1 = 8192 + (w >> 1) * 1024 + (vga * 16 + ((c >> 2) & 1) * 8 + ((rho0 + 2 + 2 * (vga & 1)) & 7)) * 16 + (c & 3) * 4;
    const unsigned vra = 8192 + (g * 16 + (c & 8) + (((c & 7) + 2 * (g & 1)) & 7)) * 16;
    const unsigned mo = w * 2048 + lane * 16;
    const unsigned ap1 = ((2 * w + 2) & 7) * 1024 + lane * 16, ap2 = ((2 * w + 4) & 7) * 1024 + lane * 16, ap3 = ((2 * w + 6) & 7) * 1024 + lane * 16;
    const unsigned uso = OFF_U + (8 * w + 2 * (g & 1) + (lane >> 5)) * 16384 + h * 256 + c * 16;
    unsigned so, hm;
    unsigned pf = 16384 + w * 2048;
    const unsigned t0 = (unsigned)__builtin_amdgcn_s_memrealtime();
    unsigned tso = lane < 32 ? OFF_TS + lane * 128 : OOR;
    const unsigned i1 = P.dly[3] * 3 / 4, i2 = (P.dly[13] - P.dly[3]) * 3 / 4;
    unsigned est = (P.dly[2] - P.dly[17]) * 8 + 4, ew = (w == 3 && lane == 0) ? OFF_TS + h * 128 + 12 : OOR;
    asm volatile(
        "s_mov_b32 s59, %[w]\n"
        VO_WLOAD AST(0) CLRWAIT(1) AST(1) ASTF CLRSO "v_add_u32 %[ew], s62, %[ew]\n"
        "s_mov_b32 s72, s63\n"
        "s_mov_b32 %[s], 0\ns_mov_b32 s79, %[t0]\n" VZERO UZERO
        SETSO("%[so]", "%[s]")
        GRID MISSUE(136)
        "s_waitcnt vmcnt(5)\n"
        SMF "v[128:131], v[136:139], a[0:7], %[idx]\n"
        "ds_write_b128 %[mo], v[136:139]\n"
        "v_max3_u32 v248, v136, v137, v138\n"
        SMF "v[132:135], v[136:139], a[8:15], %[idx]\n"
        "s_waitcnt vmcnt(4)\n"
        "ds_write_b128 %[mo], v[140:143] offset:1024\n"
        "v_max3_u32 v249, v139, v140, v141\n"
        "v_max3_u32 v248, v248, v142, v143\n"
        "v_max_u32 v248, v248, v249\n"
        "v_cmp_eq_u32 vcc, -1, v248\n"
        "s_cbranch_vccnz Lretry%=\n"
        "Lgo%=:\n" PT(1)
        MREAD(144)
        V2("v[140:143]", "16:23", "24:31")
        "s_waitcnt lgkmcnt(5)\n" V2("v[144:147]", "32:39", "40:47")
        "s_waitcnt lgkmcnt(4)\n" V2("v[148:151]", "48:55", "56:63")
        "s_waitcnt lgkmcnt(3)\n" V2("v[152:155]", "64:71", "72:79")
        "s_waitcnt lgkmcnt(2)\n" V2("v[156:159]", "80:87", "88:95")
        "s_waitcnt lgkmcnt(1)\n" V2("v[160:163]", "96:103", "104:111")
        "s_waitcnt lgkmcnt(0)\n" V2("v[164:167]", "112:119", "120:127")
        "s_add_u32 s67, s63, 0x10000\ns_nop 2\n"
        SW(128, 129) SW(130, 131) AD(128, 129) AD(130, 131)
        SW(132, 133) SW(134, 135) AD(132, 133) CV(248, 128, 132)
        "ds_write_b32 %[va0], v248\n"
        AD(134, 135) CV(249, 130, 134)
        "ds_write_b32 %[va1], v249\n"
        "s_waitcnt lgkmcnt(0)\n"
        "s_barrier\n"
        "ds_read_b128 v[168:171], %[vra]\nds_read_b128 v[172:175], %[vra] offset:1024\n"
        VZERO
        "s_waitcnt lgkmcnt(1)\n"
        UM("176:179", F0, "128:135") UM("180:183", F0, "144:151")
        "s_waitcnt lgkmcnt(0)\n"
        UM("176:179", F1, "136:143") UM("180:183", F1, "152:159")
        UM("184:187", F0, "160:167") UM("188:191", F0, "176:183")
        SW(176, 180) SW(177, 181) SW(178, 182)
        UM("184:187", F1, "168:175")
        SW(179, 183) AD(176, 180) AD(177, 181)
        UM("188:191", F1, "184:191")
        AD(178, 182) AD(179, 183) CV(240, 176, 177)
        UM("192:195", F0, "192:199")
        CV(241, 178, 179) MN(240) MN(241)
        UM("196:199", F0, "208:215")
        SW(184, 188) SW(185, 189) SW(186, 190)
        UM("192:195", F1, "200:207")
        SW(187, 191) AD(184, 188) AD(185, 189)
        UM("196:199", F1, "216:223")
        AD(186, 190) AD(187, 191) CV(242, 184, 185)
        UM("200:203", F0, "224:231")
        CV(243, 186, 187) MN(242) MN(243)
        UM("204:207", F0, "240:247")
        SW(192, 196) S16(240, 242) S16(241, 243)
        UM("200:203", F1, "232:239")
        "buffer_store_dwordx4 v[240:243], %[uso], %[srd], s63 offen sc1\n"
        SW(193, 197) SW(194, 198)
        UM("204:207", F1, "248:255")
        SW(195, 199) AD(192, 196) AD(193, 197) AD(194, 198) AD(195, 199) CV(244, 192, 193) CV(245, 194, 195) MN(244)
        SW(200, 204) SW(201, 205) SW(202, 206) SW(203, 207) AD(200, 204) AD(201, 205) AD(202, 206) AD(203, 207)
        CV(246, 200, 201) CV(247, 202, 203) MN(246) MN(247) MN(245)
        S16(244, 246) "s_nop 0\n" S16(245, 247)
        "buffer_store_dwordx4 v[244:247], %[uso], %[srd], s67 offen sc1\n" PT(2)
        "s_memrealtime s[70:71]\n"
        UBSO("s63", "s63") UBSO("s67", "s67")
        "buffer_store_dwordx4 v[240:243], %[uso], %[srd], s63 offen sc1\n"
        "buffer_store_dwordx4 v[244:247], %[uso], %[srd], s67 offen sc1\n"
        "s_cmp_eq_u32 %[s], 63\n"
        "s_cbranch_scc1 Lend%=\n"
        ESTPUB(249)
        TSLOAD
        UZERO ESTQ
        TSFOLLOW(0)
        PFLUSH
        "s_add_u32 %[s], %[s], 1\n"
        SETSO("%[so]", "%[s]")
        BIGSO("s63", "%[s]", 19, 2, RUA)
        "s_branch Lpoll%=\n"
        MRETRY(136)
        "Lend%=:\ns_waitcnt vmcnt(0)\n"
        : [s] "=&s"(s), [so] "=&s"(so), [hm] "=&s"(hm), [bud] "+s"(budget), [pf] "+v"(pf), [tso] "+v"(tso), [est] "+s"(est), [ew] "+v"(ew)
        : [rv] "s"(rv), [ro] "s"(ro), [wvo] "v"(wvo), [woo] "v"(woo),
          [srd] "s"(srd), [mo] "v"(mo), [idx] "v"(idx), [ap1] "v"(ap1), [ap2] "v"(ap2), [ap3] "v"(ap3), [va0] "v"(va0), [va1] "v"(va1),
          [vra] "v"(vra), [uso] "v"(uso), [i1] "s"(i1), [i2] "s"(i2),
          [gd] "s"(P.dly[0]), [cl] "s"(P.dly[18]), [vp] "s"(P.dly[17]), [t0] "s"(t0) ASTOP(1), [w] "s"(w)
        : ACL, CLM, "v96", "v97", "v98", "v99", CL8(10), CL8(11), CL8(12), CL8(13), CL8(14), CL8(15), CL8(16), CL8(17), CL8(18), CL8(19), CL8(20), CL8(21),
          CL8(22), CL8(23), CL8(24), "v250", "v251", "v252", "v253", "v254", "v255", "s59", SCL, "vcc", "scc", "memory");
    if (budget < 0 && lane == 0) st32<SC1>(2, rws, C_ERR, 0);
    STAMP(160 + h);
    PDUMP(32 + h);
}

#define VWALL VWL("96:99", "0", "s61") VWL("100:103", "1024", "s61") VWL("104:107", "2048", "s61") VWL("108:111", "3072", "s61") \
              VWL("112:115", "0", "s62") VWL("116:119", "1024", "s62") VWL("120:123", "2048", "s62") VWL("124:127", "3072", "s62")
#define OWP(n, r, b) \
    "s_waitcnt vmcnt(" #n ")\n" \
    SMF "v[236:239], v[" #r ":" #r "+3], v[" b "], %[idx]\n" \
    "v_max3_u32 v248, v248, v[" #r "], v[" #r "+1]\nv_max3_u32 v249, v249, v[" #r "+2], v[" #r "+3]\n"
#define OWB(r, u) \
    OWP(4, r, "96:103") OWP(3, r+4, "104:111") OWP(2, r+8, "112:119") OWP(1, r+12, "120:127") OWP(0, r+16, u) \
    "v_max_u32 v248, v248, v249\n" \
    "v_cmp_eq_u32 vcc, -1, v248\n"
#define OWC(a, so) \
    "buffer_load_dwordx4 v[168:171], %[ulo], %[srd], " so " offen sc1\n" \
    "buffer_load_dwordx4 v[172:175], %[ulo], %[srd], " so " offen offset:1024 sc1\n" \
    "buffer_load_dwordx4 v[148:151], v[" #a "], %[srd], %[so] offen sc1\n" \
    "buffer_load_dwordx4 v[152:155], v[" #a "+1], %[srd], %[so] offen sc1\n" \
    "buffer_load_dwordx4 v[156:159], v[" #a "+2], %[srd], %[so] offen sc1\n" \
    "buffer_load_dwordx4 v[160:163], v[" #a "+3], %[srd], %[so] offen sc1\n" \
    "buffer_load_dwordx4 v[164:167], v[" #a "+4], %[srd], %[so] offen sc1\n"
#define OWD \
    "s_waitcnt vmcnt(5)\n" \
    "v_max3_u32 v248, v168, v169, v170\nv_max3_u32 v249, v171, v172, v173\nv_max3_u32 v248, v248, v174, v175\n" \
    Z4(236, 237, 238, 239) \
    S16(168, 170) S16(169, 171) S16(172, 174) S16(173, 175) \
    "v_swap_b32 v169, v170\nv_swap_b32 v173, v174\n" \
    OWB(148, "168:175")
DEV void role_ow(const Params& P, rsrc_t rws, u32x4 srd, char* lds, int h, int w, int lane) {
    const int g = lane >> 4, c = lane & 15;
    int budget = SPIN_CAP, s;
    const int idx = (c < 8) ? 0x4444 : 0xEEEE;
    const unsigned ulo = OFF_U + h * 16384 + w * 2048 + lane * 16;
    u32x8 pl, pb, pc;
    {
        const int hf = (c >> 3), r = c & 7;
#pragma unroll
        for (int n = 0; n < 5; ++n) {
            unsigned o = n < 4 ? (8 * w + 2 * n + (g >> 1)) * 640 + (g & 1) * 256 + hf * 128 + r * 16
                               : (8 * w + 2 * g + hf) * 640 + 512 + r * 16;
            pl[n] = pofs(o); pb[n] = pofs(o + 20480); pc[n] = pofs(o + 40960);
        }
        pb[5] = pb[6] = pb[7] = pc[5] = pc[6] = pc[7] = 0;
        pl[5] = w * 512 + ((g >> 1) * 16 + (g & 1) * 32 + c) * 4;
        const int rr = (lane >> 2) & 7;
        pl[6] = ((rr & 1) * 16 + (rr >> 2) * 32 + ((rr >> 1) & 1) * 64 + 8 * (lane >> 5) + 2 * (lane & 3)) * 4;
        pl[7] = h * 256 + lane * 4 + (w ? w - 1 : 0) * 16384;
    }
    const unsigned vwo = OFF_VW + h * 65536 + w * 16384 + lane * 16;
    const unsigned fvo = C_VWF + ((h >> 3) * 32 + w * 8 + (lane & 7)) * 128;
    unsigned so, hm;
    unsigned pf = 16384 + w * 2048;
    const unsigned t0 = (unsigned)__builtin_amdgcn_s_memrealtime();
    unsigned tso = lane < 32 ? OFF_TS + lane * 128 : OOR, tsw = lane == 0 ? OFF_TS + h * 128 : OOR;
    unsigned seen = 0, est = (P.dly[0] - P.dly[2] - P.dly[8]) * 8 + 4;
    asm volatile(
        CLRWAIT(1) CLRSO
        "s_mov_b32 s72, s63\nv_add_u32 %[tsw], s62, %[tsw]\n"
        "s_mov_b32 s69, 0\n"
        VO_FLAG("A")
        "s_mov_b32 %[s], 0\ns_mov_b32 s79, %[t0]\n" Z4(236, 237, 238, 239)
        SETSO("%[so]", "%[s]")
        "s_xor_b32 s61, s72, 0x2000\ns_add_u32 s62, s61, 0x1000\n"
        VWALL
        "s_mov_b32 s68, 1\n" SETSO("s70", "s68")
        "s_memrealtime s[76:77]\ns_waitcnt lgkmcnt(0)\ns_mov_b32 s79, s76\ns_mov_b32 s74, s76\ns_mov_b32 s67, %[p0]\ns_sub_u32 s66, %[gd], s67\ns_mov_b32 s73, 0\n"
        GRID
        OWC(88, "s63") "s_mov_b32 s75, 0\n" OWD
        "s_cbranch_vccnz Lretry%=\n"
        "Lgo%=:\n" PT(4)
        "s_cmp_eq_u32 %[w], 1\n"
        "s_nop 1\n"
        SW(236, 237) SW(238, 239) AD(236, 237) AD(238, 239)
        "ds_write2_b32 v93, v236, v238 offset1:64\n"
        "s_waitcnt lgkmcnt(0)\n"
        "s_barrier\n"
        "s_cbranch_scc1 Lnom%=\n"
        "ds_read2_b64 v[216:219], v94 offset1:64\n"
        "ds_read2_b64 v[220:223], v94 offset0:128 offset1:192\n"
        "s_waitcnt lgkmcnt(1)\n"
        "v_pk_add_f32 v[216:217], v[216:217], v[218:219]\n"
        "s_waitcnt lgkmcnt(0)\n"
        "v_pk_add_f32 v[220:221], v[220:221], v[222:223]\n"
        "v_pk_add_f32 v[216:217], v[216:217], v[220:221]\n"
        CV(216, 216, 217) MN(216)
        "buffer_store_dword v216, v95, %[srd], s70 offen sc1\n"
        "Lnom%=:\n" PT(5)
        "s_cmp_eq_u32 %[s], 62\n"
        "s_cbranch_scc1 Lend%=\n"
        "s_cmp_lg_u32 %[w], 1\n"
        "s_cbranch_scc1 Lnot%=\n"
        "s_memrealtime s[76:77]\n"
        "s_add_u32 s65, s74, s66\n"
        "s_add_u32 s60, s65, s67\n"
        "s_sub_u32 s65, s65, %[v0]\n"
        "s_lshr_b32 s64, %[est], 3\ns_add_u32 s64, s64, %[vp]\nv_mov_b32 v217, s64\n"
        "s_waitcnt lgkmcnt(0)\n"
        "s_sub_i32 s78, s76, s65\n"
        "s_cmp_gt_i32 s78, " STR(XFAR) "\n"
        "s_cselect_b32 s65, %[gx], %[gy]\n"
        "s_add_u32 s77, s76, s65\n"
        "s_add_u32 s77, s77, s67\n"
        "s_add_u32 s77, s77, s73\n"
        "s_cmp_gt_i32 s78, 0\n"
        "s_cselect_b32 s65, %[q1], 0\n"
        "s_cmp_gt_i32 s78, %[d1]\n"
        "s_cselect_b32 s65, %[q2], s65\n"
        "s_add_u32 s60, s60, s65\n"
        "s_cmp_gt_i32 s78, %[d2]\n"
        "s_cselect_b32 s60, s77, s60\n"
        "s_cselect_b32 s65, %[sk], -0x100\n"
        "s_add_i32 s73, s73, s65\n" CLAMP("s73", 0, 160)
        "s_min_u32 s60, s60, -2\n"
        "s_lshl_b32 s65, s68, 14\n"
        "v_mov_b32 v216, s60\n"
        "buffer_store_dwordx2 v[216:217], %[tsw], %[srd], s65 offen sc1\n"
        ESTUP("s76", "s74") "s_add_i32 %[est], %[est], s60\n"
        "Lnot%=:\n"
        "s_barrier\n"
        Z4(236, 237, 238, 239)
        PFLUSH
        "s_add_u32 %[s], %[s], 1\n"
        "s_mov_b32 %[so], s70\n"
        "s_mov_b32 s69, %[s]\n"
        BIGSO("s61", "s69", 21, 0, RVW)
        "s_xor_b32 s61, s61, 0x2000\n"
        "s_add_u32 s62, s61, 0x1000\n"
        "s_mov_b32 s79, s74\n"
        WAITT("%[dr]", "Lv") PT(2)
        TSLOAD4
        "buffer_load_dword v252, off, %[srd], %[fra] sc1\n"
        "s_add_u32 s60, %[s], " STR(LEAD) "\ns_min_u32 s60, s60, 64\n"
        "s_cmp_lt_u32 %[seen], s60\n"
        "s_cselect_b32 s60, 0, 0x40000000\n"
        "buffer_load_dword v253, %[fvo], %[srd], s60 offen sc1\n"
        "s_cmp_gt_u32 %[seen], s69\n"
        "s_cbranch_scc1 LhaveB%=\n"
        VO_FLAG("B")
        VWALL
        BIGSO("s63", "%[s]", 19, 2, RUA)
        "s_add_u32 s68, %[s], 1\n" SETSO("s70", "s68")
        "s_add_u32 s79, s79, %[gd]\n"
        "s_waitcnt vmcnt(8)\n"
        VWFMIN(253, "s60")
        "s_max_u32 %[seen], %[seen], s60\n"
        "Lra%=:\n"
        "v_readfirstlane_b32 s60, v252\n"
        "s_add_u32 s60, s60, 3\n"
        "s_cmp_gt_u32 s60, %[s]\n"
        "s_cbranch_scc1 Lrad%=\n"
        "s_sleep 2\n"
        "buffer_load_dword v252, off, %[srd], %[fra] sc1\n"
        "s_sub_u32 %[bud], %[bud], 1\n"
        "s_waitcnt vmcnt(0)\n"
        "s_cmp_lt_i32 %[bud], 0\n"
        "s_cbranch_scc0 Lra%=\n"
        "Lrad%=:\n"
        TSMAX(8) PT(3)
        "s_cbranch_vccz Ltsok%=\n"
        WAITT("-45", "Lt")
        "Lts%=:\n"
        TSLOAD4 TSMAX(0)
        "s_cbranch_vccz Ltsok%=\n"
        "s_sub_u32 %[bud], %[bud], 1\n"
        "s_cmp_lt_i32 %[bud], 0\n"
        "s_cbranch_scc0 Lts%=\n"
        "Ltsok%=:\n"
        "s_add_u32 s79, s79, s76\n"
        "s_mov_b32 s74, s79\n"
        "s_branch Lpoll%=\n"
        "Lretry%=:\n"
        UBSO("s60", "s63")
        OWC(80, "s60") "s_mov_b32 s75, 1\n" OWD
        "s_cbranch_vccz Lgo%=\n"
        OWC(72, "s63") "s_mov_b32 s75, 2\n" OWD
        "s_cbranch_vccz Lgo%=\n"
        "Lspin%=:\n"
        OWC(88, "s63") "s_mov_b32 s75, 3\n" OWD
        "s_cbranch_vccz Lgo%=\n"
        "s_sub_u32 %[bud], %[bud], 1\n"
        "s_cmp_lt_i32 %[bud], 0\n"
        "s_cbranch_scc0 Lspin%=\n"
        "s_branch Lgo%=\n"
        "Lend%=:\ns_waitcnt vmcnt(0)\n"
        : [s] "=&s"(s), [so] "=&s"(so), [hm] "=&s"(hm), [bud] "+s"(budget), [seen] "+s"(seen), [pf] "+v"(pf), [tso] "+v"(tso), [tsw] "+v"(tsw), [est] "+s"(est)
        : "{v[88:95]}"(pl), "{v[80:87]}"(pb), "{v[72:79]}"(pc), [d1] "s"(P.dly[3] + 3), [d2] "s"(P.dly[15]), [q1] "s"(P.dly[3] + 13), [q2] "s"(P.dly[16]), [srd] "s"(srd), [idx] "v"(idx), [vwo] "v"(vwo), [fvo] "v"(fvo), [ulo] "v"(ulo),
          [gd] "s"(P.dly[0]), [sk] "s"(P.dly[1]), [dr] "s"(P.dly[6]), [gx] "s"(P.dly[7]), [gy] "s"(P.dly[10]), [v0] "s"(P.dly[12]), [p0] "s"(P.dly[2]), [vp] "s"(P.dly[8]), [cl] "s"(P.dly[18]), [fra] "s"(C_U2F + h * 128),
          [t0] "s"(t0), [w] "s"(w)
        : "v96", "v97", "v98", "v99", CL8(10), CL8(11), CL8(12), CL8(13), CL8(14), CL8(15), CL8(16), CL8(17), CL8(20), CL8(21),
          CL8(22), CL8(23), CL8(24), "v250", "v251", "v252", "v253", "v254", "v255", SCL, "vcc", "scc", "memory");
    if (budget < 0 && lane == 0) st32<SC1>(3, rws, C_ERR, 0);
    STAMP(320 + h);
    PDUMP(64 + h);
}

#define XDMA(o) "buffer_load_dwordx4 %[xo], %[srd], s66 offen offset:" #o " sc1 lds\n"
#define XDMA8 "s_nop 0\n" XDMA(0) XDMA(1024) XDMA(2048) XDMA(3072) "s_add_u32 m0, m0, 0x1000\ns_add_u32 s66, s66, 0x1000\n" XDMA(0) XDMA(1024) XDMA(2048) XDMA(3072)
#define AHEAD0 "s_cmp_eq_u32 %[w], 0\ns_cselect_b32 s65, %[ld], 0x100\n"
#define AHEAD(r, X0, X1) \
    "s_cmp_le_u32 s69, s65\n" \
    "s_cbranch_scc1 Lah%=\n" \
    "s_sub_u32 s60, s69, 1\nv_mov_b32 v" #r ", s60\ns_waitcnt vmcnt(0)\n" \
    "buffer_store_dword v" #r ", %[fo], %[srd], 0 offen sc1\n" \
    "Lahw%=:\n" \
    "buffer_load_dword v" #r ", off, %[srd], %[fa] sc1\n" X0 \
    "s_waitcnt vmcnt(0)\n" \
    "v_readfirstlane_b32 s65, v" #r "\n" \
    "s_add_u32 s65, s65, %[ld]\n" X1 \
    "s_cmp_le_u32 s69, s65\n" \
    "s_cbranch_scc1 Lah%=\n" \
    "s_sub_u32 %[bud], %[bud], 1\n" \
    "s_sleep 12\n" \
    "s_cmp_lt_i32 %[bud], 0\n" \
    "s_cbranch_scc0 Lahw%=\n" \
    "Lah%=:\n"
#define MINR(k) "v_min_u32_dpp v250, v250, v250 row_ror:" #k DPPM "s_nop 1\n"
#define MINL(l) "v_readlane_b32 s67, v250, " #l "\ns_min_u32 s60, s60, s67\n"
#define OUTLD "v_lshrrev_b32 v251, 2, %[l16]\ns_mov_b32 s60, " STR(C_OF_) "\nbuffer_load_dword v250, v251, %[srd], s60 offen sc1\n"
#define OUTMIN MINR(8) MINR(4) MINR(2) MINR(1) "v_readlane_b32 s60, v250, 0\n" MINL(16) MINL(32) MINL(48) \
    "s_add_u32 s60, s60, " STR(RVW) "-1\ns_min_u32 s65, s65, s60\n"
#define PUBLISH(r, n0, n1) \
    "s_cmp_lt_u32 s69, %[rk]\ns_cbranch_scc0 Lrel%=\ns_waitcnt vmcnt(" #n0 ")\nLrel%=:\n" \
    "s_cselect_b32 s60, 0, 1\ns_sub_u32 s60, s69, s60\n" \
    "v_mov_b32 v" #r ", s60\ns_waitcnt vmcnt(" #n1 ")\n" \
    "buffer_store_dword v" #r ", %[fo], %[srd], 0 offen sc1\n"
#define KQ_WISSUE \
    ".set wl_i, 0\n.rept 16\n" \
    "s_mov_b32 s62, (64*wl_t + 32*(wl_i>>3) + (wl_i&7))*8192\n" \
    "buffer_load_dwordx2 v[128+(wl_t&3)*32+wl_i*2:129+(wl_t&3)*32+wl_i*2], %[wvo], %[rw], s62 offen" WNF "\n" \
    ".set wl_i, wl_i+1\n.endr\n" ".set wl_t, wl_t+1\n"
#define KQ_CVT(a, b) \
    "v_cvt_pk_bf16_f32 v[112+kq_t*8+kq_h*4+" #a "], v[64+kq_t*16+(" #a "&1)*8+2*kq_h+(" #a ">>1)], v[68+kq_t*16+(" #a "&1)*8+2*kq_h+(" #a ">>1)]\n" \
    "v_cvt_pk_bf16_f32 v[112+kq_t*8+kq_h*4+" #b "], v[64+kq_t*16+(" #b "&1)*8+2*kq_h+(" #b ">>1)], v[68+kq_t*16+(" #b "&1)*8+2*kq_h+(" #b ">>1)]\n"
#define KQ_ST(so) "buffer_store_dwordx4 v[112+kq_t*8+kq_h*4:115+kq_t*8+kq_h*4], %[oo], %[srd], " so " offen offset:kq_h*1024 sc1\n"
#define KQ_MF(b) \
    ".if (kq_n & 15) == 0\n" MF "v[64+(kq_n>>4)*16+" #b "*4:67+(kq_n>>4)*16+" #b "*4], a[" #b "*4:" #b "*4+3], v[96+(kq_n&3)*4:99+(kq_n&3)*4], 0\n.else\n" \
    MF "v[64+(kq_n>>4)*16+" #b "*4:67+(kq_n>>4)*16+" #b "*4], a[(kq_n&15)*16+" #b "*4:(kq_n&15)*16+" #b "*4+3], v[96+(kq_n&3)*4:99+(kq_n&3)*4], v[64+(kq_n>>4)*16+" #b "*4:67+(kq_n>>4)*16+" #b "*4]\n.endif\n"
#define KQ_PACK \
    ".set wl_i, 0\n.rept 8\n" \
    ".set wl_b, 128+(wl_p&3)*32+wl_i*4\n.set wl_d, (2*wl_p+(wl_i>>2))*16+(wl_i&3)\n" \
    "v_perm_b32 v120, v[wl_b+2], v[wl_b], s76\nv_perm_b32 v121, v[wl_b+2], v[wl_b], s77\n" \
    "v_perm_b32 v122, v[wl_b+3], v[wl_b+1], s76\nv_perm_b32 v123, v[wl_b+3], v[wl_b+1], s77\n" \
    "v_accvgpr_write_b32 a[wl_d], v120\nv_accvgpr_write_b32 a[wl_d+4], v121\n" \
    "v_accvgpr_write_b32 a[wl_d+8], v122\nv_accvgpr_write_b32 a[wl_d+12], v123\n" \
    ".set wl_i, wl_i+1\n.endr\n.set wl_p, wl_p+1\n"
DEV void role_kq(const Params& P, rsrc_t rws, u32x4 srd, int h, int w, int lane) {
    const int g = lane >> 4, c = lane & 15;
    const u32x4 rw = mksrd(w < 2 ? P.wk : P.wq, WSZ);
    const unsigned wvo = 8 * g * 8192 + h * 256 + (64 * (w & 1) + 4 * c) * 2;
    const unsigned xo = OFF_XN + w * 8192 + lane * 16, l16 = lane * 16;
    const unsigned oo = (P.rolemask & 32) ? OOR : (w < 2 ? OFF_KF : OFF_QF) + h * 16384 + (w & 1) * 2048 + lane * 16;
    const unsigned fo = lane == 0 ? C_KQF + h * 128 + w * 4 : OOR;
    int budget = SPIN_CAP;
    asm volatile(
        AST(0) "s_mov_b32 s76, 0x05040100\ns_mov_b32 s77, 0x07060302\n"
        ".set wl_t, 0\n.rept 3\n" KQ_WISSUE ".endr\n"
        ".set wl_p, 0\n.rept 7\n" ASTP(1, 0) ASTP(2, 1) ASTP(3, 4)
        ".if wl_p < 6\ns_waitcnt vmcnt(32)\n.else\ns_waitcnt vmcnt(19)\n.endif\n"
        ".if wl_t < 8\n" KQ_WISSUE ".endif\n"
        ".if wl_p == 5\n" CLRA(0, 103) ".endif\n"
        KQ_PACK ".endr\n"
        AST(4) CLRB(0) AST(5)
        "s_lshl_b32 s68, %[w], 13\n"
        "s_mov_b32 m0, s68\ns_mov_b32 s66, 0\n" XDMA8
        "s_add_u32 m0, s68, 0x8000\ns_mov_b32 s66, 0x8000\n" XDMA8
        KQ_PACK
        "s_mov_b32 s69, 0\ns_mov_b32 s70, 0x40000000\ns_xor_b32 s72, s63, 0x2000\ns_mov_b32 s71, s72\n"
        "v_mov_b32 v60, %[l16]\nv_add_u32 v61, 0x8000, %[l16]\n"
        "s_waitcnt vmcnt(8)\ns_barrier\n" AST(6)
        "ds_read_b128 v[96:99], v60\nds_read_b128 v[100:103], v60 offset:1024\nds_read_b128 v[104:107], v60 offset:2048\n"
        AHEAD0
        ".p2align 6\nLloop%=:\n" ASTL(7, 2) AHEAD(63, "", "")
        ".set kq_n, 0\n.rept 32\n"
        ".if kq_n == 29\ns_barrier\n.endif\n"
        "s_waitcnt lgkmcnt(2)\n"
        KQ_MF(0)
        ".if kq_n < 29\nds_read_b128 v[96+((kq_n+3)&3)*4:99+((kq_n+3)&3)*4], v60 offset:(kq_n+3)*1024\n"
        ".else\nds_read_b128 v[96+((kq_n+3)&3)*4:99+((kq_n+3)&3)*4], v61 offset:(kq_n-29)*1024\n.endif\n"
        KQ_MF(1)
        ".if kq_n == 1 || kq_n == 2\n.set kq_t, 1\n.set kq_h, kq_n-1\n" KQ_CVT(0, 1) ".endif\n"
        ".if kq_n == 18 || kq_n == 19\n.set kq_t, 0\n.set kq_h, kq_n-18\n" KQ_CVT(0, 1) ".endif\n"
        ".if kq_n == 3\ns_add_u32 s66, s69, 2\ns_and_b32 s67, s66, 3\ns_lshl_b32 s67, s67, 15\n.endif\n"
        ".if kq_n == 8\ns_add_u32 m0, m0, 0x1000\ns_add_u32 s66, s66, 0x1000\n.endif\n"
        KQ_MF(2)
        ".if kq_n == 1 || kq_n == 2 || kq_n == 18 || kq_n == 19\n" KQ_CVT(2, 3) ".endif\n"
        ".if kq_n == 3\ns_add_u32 m0, s67, s68\ns_lshl_b32 s66, s66, 15\n.endif\n"
        ".if kq_n >= 4 && kq_n < 12\nbuffer_load_dwordx4 %[xo], %[srd], s66 offen offset:(kq_n&3)*1024 sc1 lds\n.endif\n"
        ".if kq_n == " STR(KQE) "\ns_cmp_eq_u32 s69, 1\ns_cbranch_scc0 Lnp%=\nv_mov_b32 v62, 1\ns_waitcnt vmcnt(8)\nbuffer_store_dword v62, %[fo], %[srd], 0 offen sc1\nLnp%=:\n.endif\n"
        ".if kq_n == 28\n" PUBLISH(62, 10, 15) ".endif\n"
        KQ_MF(3)
        ".if kq_n == 1 || kq_n == 2\n" KQ_ST("s70") ".endif\n"
        ".if kq_n == 18 || kq_n == 19\n" KQ_ST("s71") ".endif\n"
        ".if kq_n == 30\ns_add_u32 s70, s71, 0x1000\ns_add_u32 s69, s69, 1\n" BIGSO("s71", "s69", 19, 2, RKQ) ".endif\n"
        ".if kq_n == 31\ns_add_u32 s67, s69, 1\ns_and_b32 s67, s67, 3\ns_lshl_b32 s67, s67, 15\n.endif\n"
        ".set kq_n, kq_n+1\n.endr\n"
        "v_mov_b32 v60, v61\n"
        "v_add_u32 v61, s67, %[l16]\n"
        "s_cmp_lt_u32 s69, 64\n"
        "s_cbranch_scc1 Lloop%=\n"
        "s_nop 7\n"
        ".set kq_t, 1\n.set kq_h, 0\n" KQ_CVT(0, 1) KQ_CVT(2, 3) KQ_ST("s70")
        ".set kq_h, 1\n" KQ_CVT(0, 1) KQ_CVT(2, 3) KQ_ST("s70")
        "v_mov_b32 v62, 64\n"
        "s_waitcnt vmcnt(0) lgkmcnt(0)\n"
        "buffer_store_dword v62, %[fo], %[srd], 0 offen sc1\n" ASTF
        : [bud] "+s"(budget)
        : [rw] "s"(rw), [wvo] "v"(wvo), [srd] "s"(srd), [xo] "v"(xo), [l16] "v"(l16), [oo] "v"(oo), [fo] "v"(fo), [w] "s"(w), [ld] "s"(P.dly[9]), [rk] "s"(P.dly[20]), [fa] "s"(C_U2F + h * 128) ASTOP(2)
        : ACL, CL8(6), CL8(7), CL8(8), CL8(9), CL8(10), CL8(11), CL8(12), CL8(13), CL8(14), CL8(15), CL8(16), CL8(17), CL8(18), CL8(19), CL8(20), CL8(21),
          CL8(22), CL8(23), CL8(24), "v250", "v251", "v252", "v253", "v254", "v255", SCL, "vcc", "scc", "memory");
    if (budget < 0 && lane == 0) st32<SC1>(5, rws, C_ERR, 0);
    STAMP(192 + h);
}

#define VW_VISSUE \
    ".set wl_i, 0\n.rept 16\n" \
    "s_mov_b32 s62, (64*wl_t + 32*(wl_i>>3) + (wl_i&7))*8192\n" \
    "buffer_load_dword v[96+wl_t*16+wl_i], %[wvo], %[rv], s62 offen" WNF "\n" \
    ".set wl_i, wl_i+1\n.endr\n" ".set wl_t, wl_t+1\n"
#define VW_VPACK \
    ".set wl_i, 0\n.rept 8\n" \
    ".set wl_b, 96+wl_p*16+wl_i*2\n.set wl_d, wl_p*16+(wl_i>>2)*8+(wl_i&3)\n" \
    "v_perm_b32 v84, v[wl_b+1], v[wl_b], s76\nv_perm_b32 v85, v[wl_b+1], v[wl_b], s77\n" \
    "v_accvgpr_write_b32 a[wl_d], v84\nv_accvgpr_write_b32 a[wl_d+4], v85\n" \
    ".set wl_i, wl_i+1\n.endr\n.set wl_p, wl_p+1\n"
#define VW_OISSUE(p, r) \
    ".set wl_i, 0\n.rept 16\n" \
    "s_mov_b32 s72, (64*" #p " + 32*(wl_i>>3) + (wl_i&4))*1024\n" \
    "buffer_load_dwordx4 v[" #r "+wl_i*4:" #r "+3+wl_i*4], %[woo], %[ro], s72 offen offset:(wl_i&3)*1024" WNF "\n" \
    ".set wl_i, wl_i+1\n.endr\n"
#define VW_OPACK(p, r) \
    ".set wl_i, 0\n.rept 8\n.set wl_k, 0\n.rept 4\n" \
    ".set wl_b, " #r "+wl_i*8+wl_k\n.set wl_d, 128+wl_k*32+" #p "*8+wl_i\n" \
    "v_perm_b32 v84, v[wl_b+4], v[wl_b], s76\nv_perm_b32 v85, v[wl_b+4], v[wl_b], s77\n" \
    "v_accvgpr_write_b32 a[wl_d], v84\nv_accvgpr_write_b32 a[wl_d+16], v85\n" \
    ".set wl_k, wl_k+1\n.endr\n.set wl_i, wl_i+1\n.endr\n"
#define VW_CVT(a, b) \
    "v_cvt_pk_bf16_f32 v[224+(vw_i&3)*4+" #a "], v[160+vw_i*8+" #a "], v[164+vw_i*8+" #a "]\n" \
    "v_cvt_pk_bf16_f32 v[224+(vw_i&3)*4+" #b "], v[160+vw_i*8+" #b "], v[164+vw_i*8+" #b "]\n"
#define VW_ST(so) "s_add_u32 s72, " so ", vw_i*0x10000\nbuffer_store_dwordx4 v[224+(vw_i&3)*4:227+(vw_i&3)*4], %[oo], %[srd], s72 offen sc1\n"
#define VW_VC(t) \
    "v_cvt_pk_bf16_f32 v[240+" #t "*4], v[112+" #t "*8], v[116+" #t "*8]\nv_cvt_pk_bf16_f32 v[241+" #t "*4], v[113+" #t "*8], v[117+" #t "*8]\n" \
    "v_cvt_pk_bf16_f32 v[242+" #t "*4], v[114+" #t "*8], v[118+" #t "*8]\nv_cvt_pk_bf16_f32 v[243+" #t "*4], v[115+" #t "*8], v[119+" #t "*8]\n" \
    "ds_write_b128 v62, v[240+" #t "*4:243+" #t "*4] offset:" #t "*4096\n"
#define VW_MV(b) \
    ".if (vw_n & 15) == 0\n" MF "v[112+(vw_n>>4)*8+" #b "*4:115+(vw_n>>4)*8+" #b "*4], a[" #b "*4:" #b "*4+3], v[96+(vw_n&3)*4:99+(vw_n&3)*4], 0\n.else\n" \
    MF "v[112+(vw_n>>4)*8+" #b "*4:115+(vw_n>>4)*8+" #b "*4], a[(vw_n&15)*8+" #b "*4:(vw_n&15)*8+" #b "*4+3], v[96+(vw_n&3)*4:99+(vw_n&3)*4], v[112+(vw_n>>4)*8+" #b "*4:115+(vw_n>>4)*8+" #b "*4]\n.endif\n"
#define VW_MO \
    ".if vw_k == 0\n" MF "v[160+vw_a*8+vw_t*4:163+vw_a*8+vw_t*4], v[128+vw_t*4:131+vw_t*4], a[128+vw_a*16:131+vw_a*16], 0\n.else\n" \
    MF "v[160+vw_a*8+vw_t*4:163+vw_a*8+vw_t*4], v[128+vw_k*8+vw_t*4:131+vw_k*8+vw_t*4], a[128+vw_a*16+vw_k*4:131+vw_a*16+vw_k*4], v[160+vw_a*8+vw_t*4:163+vw_a*8+vw_t*4]\n.endif\n"
#define VW_VF(k) "ds_read_b128 v[128+" #k "*8:131+" #k "*8], v63 offset:" #k "*1024\nds_read_b128 v[132+" #k "*8:135+" #k "*8], v63 offset:4096+" #k "*1024\n"
#define VW_BODY \
    ".set vw_n, 0\n.rept 32\n" \
    ".if vw_n < 3\ns_waitcnt lgkmcnt(4)\n.elseif vw_n < 30\ns_waitcnt lgkmcnt(2)\n.elseif vw_n == 30\ns_waitcnt lgkmcnt(1)\n.else\ns_waitcnt lgkmcnt(0)\n.endif\n" \
    VW_MV(0) \
    ".if vw_n < 29\nds_read_b128 v[96+((vw_n+3)&3)*4:99+((vw_n+3)&3)*4], v60 offset:(vw_n+3)*1024\n.endif\n" \
    ".if vw_f == 0\n" \
    ".if vw_n >= 1 && vw_n <= 8\n.set vw_i, 4+((vw_n-1)>>1)\n.elseif vw_n == 10 || vw_n == 11\n.set vw_i, 0\n.elseif vw_n == 18 || vw_n == 19\n.set vw_i, 1\n" \
    ".elseif vw_n == 26 || vw_n == 27\n.set vw_i, 2\n.else\n.set vw_i, -1\n.endif\n" \
    ".if vw_n <= 8\n.set vw_e, 1-(vw_n&1)\n.else\n.set vw_e, vw_n&1\n.endif\n" \
    ".if vw_i >= 0 && vw_e == 0\n" VW_CVT(0, 1) ".endif\n" \
    ".set vw_a, vw_n>>3\n.set vw_k, (vw_n>>1)&3\n.set vw_t, vw_n&1\n" VW_MO \
    ".if vw_i >= 0 && vw_e == 0\n" VW_CVT(2, 3) ".endif\n" \
    ".if vw_i >= 4 && vw_e != 0\n" VW_ST("s70") ".endif\n" \
    ".if vw_i >= 0 && vw_i < 4 && vw_e != 0\n" VW_ST("s71") ".endif\n" \
    ".endif\n" \
    ".if vw_n == 9\ns_mov_b32 m0, s73\n.endif\n" \
    ".if vw_n == 16\ns_add_u32 m0, m0, 0x1000\ns_add_u32 s66, s66, 0x1000\n.endif\n" \
    VW_MV(1) \
    ".if vw_n >= 12 && vw_n < 20\nbuffer_load_dwordx4 %[xo], %[srd], s66 offen offset:(vw_n&3)*1024 sc1 lds\n.endif\n" \
    ".if vw_n == 18\n" VW_VC(0) ".endif\n" \
    ".set vw_n, vw_n+1\n.endr\n" \
    ".set vw_n, 0\n.rept 32\n" \
    ".set vw_a, 4+((vw_n>>1)&3)\n.set vw_k, vw_n>>3\n.set vw_t, vw_n&1\n" \
    ".if vw_f == 0\n" VW_MO ".endif\n" \
    ".if vw_n == 1\ns_nop 5+2*vw_f\n.endif\n" \
    ".if vw_n == 2\n" VW_VC(1) ".endif\n" \
    ".if vw_n == 3 && vw_f == 0\n.set vw_i, 3\n" VW_CVT(0, 1) VW_CVT(2, 3) ".endif\n" \
    ".if vw_n == 4 && vw_f == 0\n" VW_ST("s71") ".endif\n" \
    ".if vw_n == 5\n.if vw_f\ns_waitcnt vmcnt(8)\n.else\n" PUBLISH(248, 12, 19) ".endif\ns_waitcnt lgkmcnt(0)\ns_barrier\n.endif\n" \
    ".if vw_n == 8\n" VW_VF(0) ".endif\n.if vw_n == 16\n" VW_VF(1) ".endif\n.if vw_n == 24\n" VW_VF(2) ".endif\n" \
    ".if vw_n == 10\ns_mov_b32 s70, s71\ns_add_u32 s71, s71, 0x200000\ns_xor_b32 s71, s71, 0x2000\ns_and_b32 s71, s71, (" STR(RVW) "+1)*0x200000-1\ns_add_u32 s69, s69, 1\n.endif\n" \
    ".if vw_n == 11\ns_mov_b32 s73, s74\ns_mov_b32 s74, s75\ns_sub_u32 s75, m0, 0x1000\n.endif\n" \
    ".if vw_n == 12\ns_add_u32 s66, s66, 0x7000\ns_sub_u32 s67, s74, s68\n.endif\n" \
    ".if vw_n == 13\nv_add_u32 v60, s67, %[l16]\nv_xor_b32 v62, 0x2000, v62\n.endif\n" \
    ".if vw_n == 26 || vw_n == 28 || vw_n == 30\nds_read_b128 v[96+((vw_n-26)>>1)*4:99+((vw_n-26)>>1)*4], v60 offset:((vw_n-26)>>1)*1024\n.endif\n" \
    ".set vw_n, vw_n+1\n.endr\n" \
    VW_VF(3) \
    "v_xor_b32 v63, 0x2000, v63\n"
DEV void role_vw(const Params& P, rsrc_t rws, u32x4 srd, int h, int w, int lane) {
    const int g = lane >> 4, c = lane & 15;
    const u32x4 rv = mksrd(P.wv, WSZ), ro = mksrd(P.wo, WSZ);
    const unsigned wvo = 8 * g * 8192 + h * 256 + (32 * w + 2 * c) * 2;
    const unsigned woo = (h * 128 + 8 * g) * 1024 + (128 * w + 8 * c) * 2;
    const unsigned xo = OFF_XN + w * 8192 + lane * 16, l16 = lane * 16;
    const unsigned oo = (P.rolemask & 64) ? OOR : OFF_VW + 8 * w * 65536 + (h >> 3) * 16384 + (h & 7) * 1024 + lane * 16;
    const unsigned fo = lane == 0 ? C_VWF + (w * 32 + h) * 128 : OOR;
    int budget = SPIN_CAP;
    asm volatile(
        AST(0) "s_mov_b32 s76, 0x05040100\ns_mov_b32 s77, 0x07060302\n"
        VW_OISSUE(0, 192) ".set wl_t, 0\n" VW_VISSUE VW_VISSUE
        "s_waitcnt vmcnt(32)\n" AST(1) VW_VISSUE VW_OPACK(0, 192) AST(2)
        ".set wl_p, 0\n.rept 7\n" ASTP(3, 4)
        ".if wl_p < 6\ns_waitcnt vmcnt(32)\n.else\ns_waitcnt vmcnt(35)\n.endif\n"
        ".if wl_t < 8\n" VW_VISSUE ".endif\n"
        ".if wl_p == 5\n" CLRA(0, 247) VW_OISSUE(1, 96) ".endif\n"
        VW_VPACK ".endr\n"
        AST(4) CLRB(16) AST(5)
        "s_lshl_b32 s68, %[w], 13\n"
        "s_mov_b32 m0, s68\ns_mov_b32 s66, 0\n" XDMA8
        "s_add_u32 m0, s68, 0x8000\ns_mov_b32 s66, 0x8000\n" XDMA8
        VW_VPACK "s_waitcnt vmcnt(16)\n" VW_OPACK(1, 96)
        "s_add_u32 s73, s68, 0x10000\ns_mov_b32 s74, s68\ns_add_u32 s75, s68, 0x8000\ns_mov_b32 s66, 0x10000\n"
        "s_mov_b32 s69, -1\ns_sub_u32 s71, s63, 0x200000\n"
        "s_lshl_b32 s67, %[w], 10\ns_add_u32 s67, s67, 0x18000\n"
        "v_mov_b32 v60, %[l16]\nv_add_u32 v62, s67, %[l16]\nv_add_u32 v63, 0x18000, %[l16]\n"
        "s_waitcnt vmcnt(8)\ns_barrier\n" AST(6)
        "ds_read_b128 v[96:99], v60\nds_read_b128 v[100:103], v60 offset:1024\nds_read_b128 v[104:107], v60 offset:2048\n"
        "s_waitcnt lgkmcnt(0)\n"
        ".set vw_f, 1\n" VW_BODY
        "s_mov_b32 s70, 0x40000000\n" AHEAD0
        ".p2align 6\nLloop%=:\n" ASTL(7, 1) AHEAD(249, OUTLD, OUTMIN)
        ".set vw_f, 0\n" VW_BODY
        "s_cmp_lt_u32 s69, 64\n"
        "s_cbranch_scc1 Lloop%=\n"
        "s_nop 7\n"
        ".set vw_i, 4\n.rept 4\n" VW_CVT(0, 1) VW_CVT(2, 3) VW_ST("s70") ".set vw_i, vw_i+1\n.endr\n"
        "v_mov_b32 v248, 64\n"
        "s_waitcnt vmcnt(0) lgkmcnt(0)\n"
        "buffer_store_dword v248, %[fo], %[srd], 0 offen sc1\n" ASTF
        : [bud] "+s"(budget)
        : [rv] "s"(rv), [ro] "s"(ro), [wvo] "v"(wvo), [woo] "v"(woo), [srd] "s"(srd), [xo] "v"(xo), [l16] "v"(l16), [oo] "v"(oo), [fo] "v"(fo), [w] "s"(w), [ld] "s"(P.dly[14]), [rk] "s"(P.dly[19]), [fa] "s"(C_U2F + h * 128) ASTOP(3)
        : ACL, CL8(6), CL8(7), CL8(8), CL8(9), CL8(10), CL8(11), CL8(12), CL8(13), CL8(14), CL8(15), CL8(16), CL8(17), CL8(18), CL8(19), CL8(20), CL8(21),
          CL8(22), CL8(23), CL8(24), "v250", "v251", "v252", "v253", "v254", "v255", SCL, "vcc", "scc", "memory");
    if (budget < 0 && lane == 0) st32<SC1>(6, rws, C_ERR, 0);
    STAMP(224 + h);
}

struct TrueT { static constexpr bool value = true; };
struct FalseT { static constexpr bool value = false; };
DEV float bfr(float v) { return (float)(__bf16)v; }
DEV void role_ffn(unsigned tk, const Params& P, rsrc_t rws, char* lds, int s, int w, int lane, u32x2 ep, int& budget) {
    const int g = lane >> 4, c = lane & 15;
    u32x2 tag;
    const rsrc_t rx = mkrs(P.x, 2048 * 512 * 2);
    const int j = s >> 2;
    const rsrc_t rkq = mkrs(j < 8 ? P.wk : P.wq, 4u << 20), rv = mkrs(P.wv, 4u << 20);
    const unsigned wpo = (threadIdx.x >> 4) * 8192 + (s & 3) * 2048 + (threadIdx.x & 15) * 128;
    {
        const int r8 = lane & 7, q = lane >> 3;
        u32x4 xv[8];
#pragma unroll
        for (int i = 0; i < 8; ++i) xv[i] = ld128<0>(rx, (32 * s + 8 * w + r8) * 1024 + q * 16 + i * 128, 0);
        f32x2 v[32];
#pragma unroll
        for (int i = 0; i < 8; ++i)
#pragma unroll
            for (int k = 0; k < 4; ++k) v[4 * i + k] = f32x2{asf(xv[i][k] << 16), asf(xv[i][k] & 0xFFFF0000u)};
        auto rowred = [](float a) { a += dppf<0x128>(a); a = add16(a); return fold32(a, a); };
        f32x2 sm = v[0];
#pragma unroll
        for (int k = 1; k < 32; ++k) sm += v[k];
        const float mean = rowred(sm.x + sm.y) * (1.0f / 512.0f);
        f32x2 sq = {0.f, 0.f};
#pragma unroll
        for (int k = 0; k < 32; ++k) { const f32x2 d = v[k] - mean; sq = __builtin_elementwise_fma(d, d, sq); }
        const float var = bfr(rowred(sq.x + sq.y) * (1.0f / 512.0f));
        const float mb = bfr(mean);
        const float den = bfr(sqrtf(bfr(var + 1e-5f)));
        const float rc = 1.0f / den;
        const unsigned dst = (w >> 1) * 16384 + (q >> 2) * 1024 + ((q & 3) * 16 + (w & 1) * 8 + r8) * 16;
#pragma unroll
        for (int i = 0; i < 8; ++i) {
            u32x4 o;
#pragma unroll
            for (int k = 0; k < 4; ++k) {
                const f32x2 d = v[4 * i + k] - mb;
                const unsigned db = pk(d.x, d.y);
                const f32x2 a = {asf(db << 16), asf(db & 0xFFFF0000u)};
                f32x2 qv = a * rc;
                const f32x2 rem = __builtin_elementwise_fma(-qv, f32x2{den, den}, a);
                qv = __builtin_elementwise_fma(rem, f32x2{rc, rc}, qv);
                o[k] = pk(qv.x, qv.y);
            }
            st128<SC1>(o, rws, OFF_XN + dst + i * 2048, s * 32768);
            *(u32x4*)(lds + dst + i * 2048) = o;
        }
        if (threadIdx.x < 32) st128<SC1>(u32x4{0, 0, 0, 0}, rws, C_CNT + s * 512 + threadIdx.x * 16, 0);
        tag = next_tag(ep);
        WAIT_VM0();
        BAR();
        if (threadIdx.x == 0) st64<SC1>(u32x2{tag.x | 0xffffu, tag.y}, rws, C_XF + (s << TSH), 0);
    }
    STAMP(960 + s);
    const unsigned p2 = ld32<0>(rv, (P.dly[21] & 1) ? wpo + j * 16 * 8192 : OOR, 0), p3 = ld32<0>(rv, (P.dly[21] & 2) ? wpo + (256 + j * 16) * 8192 : OOR, 0);
    const unsigned p4 = ld32<0>(rkq, (P.dly[21] & 4) ? wpo + (256 + (j & 7) * 16) * 8192 : OOR, 0), p5 = ld32<0>(rkq, (P.dly[21] & 8) ? wpo + (384 + (j & 7) * 16) * 8192 : OOR, 0);
    SCHED();
    const rsrc_t r1 = mkrs(P.w1, (P.rolemask & 256) ? 0 : 512 * 1376 * 2), r2 = mkrs(P.w2, (P.rolemask & 256) ? 0 : 1376 * 512 * 2);
    const unsigned vo2 = 32 * g * 1024 + (128 * w + 8 * c) * 2;
    u32x4 raw[4][8];
    f32x4 acc[8][2];
    const int rot = s >> 2, rot2 = (rot * 11) >> 2;
    auto issue1 = [&](int u, int sl) {
        const unsigned col = (w + 4 * (u >> 4)) * 128 + 8 * c;
        const unsigned vo = col < 1376 ? 8 * g * 2752 + col * 2 : OOR;
#pragma unroll
        for (int j = 0; j < 8; ++j) raw[sl][j] = ld128<0>(r1, vo, (((u + rot) & 15) * 32 + j) * 2752);
    };
    auto issue2 = [&](int k0, int sl) {
        const int k = k0 < 44 ? (k0 + rot2 < 44 ? k0 + rot2 : k0 + rot2 - 44) : 44;
#pragma unroll
        for (int j = 0; j < 8; ++j) raw[sl][j] = ld128<0>(r2, vo2 + (j & 3) * 1024, ((k >> 2) * 128 + (k & 3) * 8 + (j & 4)) * 1024);
    };
    auto frags = [&](int sl, u32x4 (&F)[8]) {
#pragma unroll
        for (int jj = 0; jj < 4; ++jj)
#pragma unroll
            for (int i = 0; i < 4; ++i) { F[2 * i][jj] = plo(raw[sl][2 * jj][i], raw[sl][2 * jj + 1][i]); F[2 * i + 1][jj] = phi(raw[sl][2 * jj][i], raw[sl][2 * jj + 1][i]); }
    };
    auto zero = [&]() {
#pragma unroll
        for (int i = 0; i < 8; ++i) { acc[i][0] = f32x4{0, 0, 0, 0}; acc[i][1] = f32x4{0, 0, 0, 0}; }
    };
    const int nst = w < 3 ? 48 : 32;
#pragma unroll
    for (int i = 0; i < 4; ++i) issue1(i, i);
    SCHED();
    asm volatile("" :: "v"(p2), "v"(p3), "v"(p4), "v"(p5));
    zero();
    auto group = [&](int u, auto last) {
#pragma unroll
        for (int i = 0; i < 4; ++i) {
            SCHED();
            const u32x4 x0 = *(const u32x4*)(lds + ((u + i + rot) & 15) * 1024 + lane * 16), x1 = *(const u32x4*)(lds + 16384 + ((u + i + rot) & 15) * 1024 + lane * 16);
            u32x4 F[8];
            frags(i, F);
            if constexpr (decltype(last)::value) issue2(i, i); else issue1(u + 4 + i, i);
#pragma unroll
            for (int n = 0; n < 8; ++n) { acc[n][0] = mfma(F[n], x0, acc[n][0]); acc[n][1] = mfma(F[n], x1, acc[n][1]); }
        }
    };
    auto hidden = [&](int u) {
        char* hb = lds + 32768 + (w + 4 * (u >> 4)) * 8192 + lane * 16;
#pragma unroll
        for (int r = 0; r < 4; ++r)
#pragma unroll
            for (int t = 0; t < 2; ++t) {
                u32x4 d;
#pragma unroll
                for (int n = 0; n < 4; ++n) d[n] = pk(fmaxf(acc[2 * n][t][r], 0.f), fmaxf(acc[2 * n + 1][t][r], 0.f));
                *(u32x4*)(hb + r * 2048 + t * 1024) = d;
            }
        zero();
    };
#pragma nounroll
    for (int u = 0; u < nst - 4; u += 4) {
        group(u, FalseT());
        if ((u & 15) == 12) hidden(u);
    }
    group(nst - 4, TrueT());
    hidden(nst - 4);
    BAR();
    STAMP(832 + s);
#pragma nounroll
    for (int k = 0; k < 44; k += 4) {
#pragma unroll
        for (int i = 0; i < 4; ++i) {
            SCHED();
            const int kr = k + i + rot2 < 44 ? k + i + rot2 : k + i + rot2 - 44;
            const u32x4 h0 = *(const u32x4*)(lds + 32768 + kr * 2048 + lane * 16), h1 = *(const u32x4*)(lds + 33792 + kr * 2048 + lane * 16);
            u32x4 F[8];
            frags(i, F);
            issue2(k + 4 + i, i);
#pragma unroll
            for (int n = 0; n < 8; ++n) { acc[n][0] = mfma(h0, F[n], acc[n][0]); acc[n][1] = mfma(h1, F[n], acc[n][1]); }
        }
    }
#pragma unroll
    for (int t = 0; t < 2; ++t)
#pragma unroll
        for (int r = 0; r < 4; ++r) {
            const unsigned tok = 32 * s + 16 * t + 4 * g + r;
            const u32x4 xv = ld128<0>(rx, tok * 1024 + (128 * w + 8 * c) * 2, 0);
            u32x4 o0, o1;
#pragma unroll
            for (int i = 0; i < 2; ++i) {
                o0[2 * i] = asu(acc[2 * i][t][r] + asf(xv[i] << 16)); o0[2 * i + 1] = asu(acc[2 * i + 1][t][r] + asf(xv[i] & 0xFFFF0000u));
                o1[2 * i] = asu(acc[4 + 2 * i][t][r] + asf(xv[2 + i] << 16)); o1[2 * i + 1] = asu(acc[5 + 2 * i][t][r] + asf(xv[2 + i] & 0xFFFF0000u));
            }
            st128<SC1>(u32x4{pk(asf(o0.x), asf(o0.y)), pk(asf(o0.z), asf(o0.w)), pk(asf(o1.x), asf(o1.y)), pk(asf(o1.z), asf(o1.w))}, mkrs(P.out, 2048 * 512 * 2),
                       tok * 1024 + (128 * w + 8 * c) * 2, 0);
        }
    wait_tags(rws, C_XF + (lane << TSH), tag, budget);
    WAIT_VM0();
    BAR();
    if (threadIdx.x == 0) st32<SC1>(1, rws, C_BF + s * 4, 0);
    STAMP(256 + s);
}

DEV void role_out(unsigned tk, const Params& P, rsrc_t rws, char* lds, int id, int w, int lane, u32x2 ep, int& budget) {
    const int g = lane >> 4, c = lane & 15;
    const int set = id >> 4, tt = (id >> 3) & 1, cg = id & 7;
    const int j0 = 8 * (cg >> 1) + 4 * (cg & 1);
    const int col = 128 * (cg >> 1) + 8 * c + 4 * (cg & 1);
    unsigned seen = 0;
    const rsrc_t ro = mkrs(P.out, 2048 * 512 * 2);
    const u32x2 tag = next_tag(ep);
    const unsigned h2 = (RFL(wait_tags(rws, C_HM, tag, budget)) >> 10) * 8192;
#pragma nounroll
    for (;;) {
        FRESH();
        const unsigned v = ld32<SC1>(rws, C_BF + lane * 4, 0);
        if (__all(v == 1u) || --budget < 0) break;
        __builtin_amdgcn_s_sleep(16);
    }
    asm volatile("" ::: "memory");
#pragma nounroll
    for (int s = set; s < NSEG; s += 4) {
        if (seen <= (unsigned)s) seen = wait_flags<8>(rws, C_VWF + ((cg >> 1) * 32 + w * 8) * 128, s, budget, lane);
        u32x4 VW[4][8];
#pragma unroll
        for (int i = 0; i < 4; ++i)
#pragma unroll
            for (int hh = 0; hh < 8; ++hh)
                VW[i][hh] = ld128<SC1>(rws, OFF_VW + (j0 + i) * 65536 + w * 16384 + (hh & 4) * 1024 + lane * 16 + (hh & 3) * 1024, (s & RVW) * 2 * MB + (h2 ^ ((~s & 1) << 13)));
        const unsigned tok = 32 * s + 16 * tt + 4 * g + w;
        const u32x2 b2 = ld64<SC1>(ro, tok * 1024 + col * 2, 0);
        const u32x4 bs = {b2.x << 16, b2.x & 0xFFFF0000u, b2.y << 16, b2.y & 0xFFFF0000u};
        if (s >= 60) STAMP(704 + id);
        if (w == 0) {
            const unsigned need = s < 59 ? s + 2 : s + 1, fa = s < 59 ? C_U2F + (id & 31) * 128 : C_PF + (lane & 31) * 128;
#pragma nounroll
            for (;;) {
                FRESH();
                const unsigned v = ld32<SC1>(rws, fa, 0);
                if (__all(v >= need) || --budget < 0) break;
                if (s >= 62 && __all(v >= 62u)) __builtin_amdgcn_s_sleep(3);
                else __builtin_amdgcn_s_sleep(36);
            }
        }
        BAR();
        if (s >= 60) STAMP(768 + id);
        const unsigned uo = OFF_U + (s & RUA) * 524288 + (h2 ^ ((s & 4) << 11)) + j0 * 16384, us = 16384;
        u32x4 U[4][2];
#define ULD(i) U[i][0] = ld128<SC1>(rws, 2 * w * 1024 + lane * 16, uo + i * us); U[i][1] = ld128<SC1>(rws, 2 * w * 1024 + 1024 + lane * 16, uo + i * us);
        ULD(0) ULD(1) ULD(2) ULD(3)
        auto umax = [&]() {
            return max(max(max(umax4(U[0][0]), umax4(U[0][1])), max(umax4(U[1][0]), umax4(U[1][1]))),
                       max(max(umax4(U[2][0]), umax4(U[2][1])), max(umax4(U[3][0]), umax4(U[3][1]))));
        };
        u32x4 Px[8], Pm[2];
        auto pld = [&]() {
#pragma unroll
            for (int hh = 0; hh < 8; ++hh) Px[hh] = ld128<SC1>(rws, OFF_P + tt * 40960 + (8 * w + (hh & 4)) * 1024 + lane * 16 + (hh & 3) * 1024, s * 81920);
#pragma unroll
            for (int n = 0; n < 2; ++n) Pm[n] = ld128<SC1>(rws, OFF_P + tt * 40960 + (32 + 2 * w + n) * 1024 + lane * 16, s * 81920);
        };
        pld();
        f32x4 acc[4];
#pragma unroll
        for (int i = 0; i < 4; ++i) acc[i] = f32x4{0, 0, 0, 0};
#pragma unroll
        for (int hh = 0; hh < 8; ++hh)
#pragma unroll
            for (int i = 0; i < 4; ++i) acc[i] = mfma(Px[hh], VW[i][hh], acc[i]);
        if (s >= 59) {
#pragma nounroll
            for (;;) {
                if (__all(umax() != ~0u) || --budget < 0) break;
                FRESH();
                ULD(0) ULD(1) ULD(2) ULD(3)
            }
        }
#pragma unroll
        for (int n = 0; n < 2; ++n)
#pragma unroll
            for (int i = 0; i < 4; ++i) acc[i] = mfma(Pm[n], U[i][n], acc[i]);
#pragma unroll
        for (int v = 0; v < 4; ++v) *(f32x4*)(lds + (w * 4 + v) * 1024 + lane * 16) = f32x4{acc[0][v], acc[1][v], acc[2][v], acc[3][v]};
        BAR();
        if (threadIdx.x == 0) st32<SC1>((s & ~3) + 4, rws, C_OF + id * 4, 0);
        f32x4 t = __builtin_bit_cast(f32x4, bs);
#pragma unroll
        for (int o = 0; o < 4; ++o) t += *(const f32x4*)(lds + (o * 4 + w) * 1024 + lane * 16);
        st64<OUTAUX>(u32x2{pk(t[0], t[1]), pk(t[2], t[3])}, ro, tok * 1024 + col * 2, 0);
    }
    if (id == 63 && threadIdx.x == 0) st64<SC1>(tag, rws, C_EPOCH, 0);
    if (budget < 0 && lane == 0) st32<SC1>(4, rws, C_ERR, 0);
    STAMP(640 + id);
}

#define PRE(v, o) "buffer_load_dwordx4 " v ", %[srd], " o " offen sc1 lds\n"
DEV void role_cl(const Params& P, rsrc_t rws, u32x4 srd, char* lds, int id, int w, int lane) {
    const int tid = w * 64 + lane;
    int budget = SPIN_CAP;
    const u32x2 ep = ld64<SC1>(rws, C_EPOCH, 0);
    const u32x4 sent = {~0u, ~0u, ~0u, ~0u};
    u32x2 tag;
    unsigned hm, h2;
    if (P.rolemask & 2) {
    unsigned m0v;
    {
        const unsigned short* m0 = (const unsigned short*)P.mem0;
        const int r = (lane & 31) >> 2, c0 = 8 * (lane >> 5) + 2 * (lane & 3);
        const int col = 128 * (id >> 3) + 8 * c0 + (id & 7);
        m0v = nosent((unsigned)m0[r * 512 + col] | ((unsigned)m0[r * 512 + col + 8] << 16));
    }
    {
        int best[2] = {1 << 30, 1 << 30};
        const unsigned reg = ((4 * id + w) % 10) * 2 * MB + MB + ((4 * id + w) / 10) * 65536 + lane * 4;
#pragma nounroll
        for (int i = 0; i < NPROBE; ++i) {
            const long long t0 = __builtin_amdgcn_s_memtime();
            st32<SC1>(~0u, rws, reg + (i & 1) * 8192, 0);
            WAIT_VM0();
            const int dt = (int)(__builtin_amdgcn_s_memtime() - t0);
            if (i & 1) best[1] = min(best[1], dt); else best[0] = min(best[0], dt);
        }
        if (lane == 0) *(unsigned*)(lds + w * 4) = best[0] < best[1] ? 0u : 1u << w;
    }
    BAR();
    tag = next_tag(ep);
    {
        const u32x4 a = *(const u32x4*)lds;
        if (tid == 0) st64<SC1>(u32x2{tag.x | a.x | a.y | a.z | a.w, tag.y}, rws, C_VOTE + (id << TSH), 0);
    }
    STAMP(64 + id);
    hm = close_halves(rws, tag, lane, budget);
    h2 = (hm >> 10) * 8192;
    if (id == 0 && tid == 0) st64<SC1>(u32x2{tag.x | hm, tag.y}, rws, C_HM, 0);
    if (w < 3) {
        st32<SC1>(m0v, rws, OFF_COMM + (hm & 1) * 8192 + w * 16384 + id * 256 + lane * 4, 0);
        st32<SC1>(m0v, rws, OFF_COMM + (hm & 1) * 8192 + w * 16384 + id * 256 + lane * 4, 49152);
    }
#pragma unroll
    for (int i = 0; i < 16; ++i) st128<SC1>(sent, rws, OFF_U + h2 + id * 16384 + tid * 16, (i >> 2) * 524288 + (i & 1) * 4096 + (i & 2) * 16 * MB);
#pragma unroll
    for (int i = 0; i < 6; ++i) reset_piece(rws, i, id, tid, hm);
    STAMP(96 + id);
    WAIT_VM0();
    BAR();
    if (tid == 0) st64<SC1>(u32x2{tag.x | hm, tag.y}, rws, C_MSG + (id << TSH), 0);
    STAMP(352 + id);
    }
    if (!(P.rolemask & 1)) return;
    tag = next_tag(ep);
    hm = RFL(wait_tags(rws, C_MSG + ((lane & 31) << TSH), tag, budget));
    h2 = (hm >> 10) * 8192;
    wait_tags(rws, C_XF + (lane << TSH), tag, budget);
    int pk_ = 0, pv = 0;
    unsigned outp = 0;
    auto fetch = [&](int i) {
        pk_ = max(pk_, i + 2); pv = max(pv, i + 2);
        const int pd = i < (int)P.dly[22] ? 2 : (int)P.dly[5];
#pragma unroll
        for (int r = 0; r < 2; ++r) {
            const bool gk = pk_ <= i + pd, gv = pv <= i + pd;
            const unsigned ko = gk ? OFF_KF + (pk_ & RKQ) * 524288 + (h2 ^ 8192 ^ ((pk_ & 4) << 11)) + id * 16384 + tid * 16 : OOR;
            const unsigned vo = gv ? OFF_VW + (pv & RVW) * 2 * MB + (h2 ^ ((~pv & 1) << 13)) + id * 65536 + tid * 16 : OOR;
            pk_ += gk; pv += gv;
            if (r == 0 || gk || gv)
                asm volatile("s_mov_b32 m0, 0x1000\ns_nop 0\n"
                             PRE("%[v]", "0") PRE("%[v]", "%[o1]") PRE("%[v]", "%[o4]") PRE("%[v]", "%[o5]")
                             PRE("%[x]", "0") PRE("%[x]", "%[o1]") PRE("%[x]", "%[o4]") PRE("%[x]", "%[o5]")
                             PRE("%[k]", "0") PRE("%[k]", "%[o1]") PRE("%[q]", "0") PRE("%[q]", "%[o1]")
                             :: [k] "v"(ko), [q] "v"(ko + 32 * MB), [v] "v"(vo), [x] "v"(vo + 32768), [srd] "s"(srd), [o1] "s"(4096), [o4] "s"(16384), [o5] "s"(20480) : "memory");
            if (r == 0) {
                asm volatile("s_waitcnt vmcnt(12)" ::: "memory");
                BAR();
                if (tid == 0) st32<SC1>(i + 1, rws, C_U2F + id * 128, 0);
            }
        }
    };
    unsigned T = (unsigned)__builtin_amdgcn_s_memrealtime() - P.dly[11];
#pragma nounroll
    for (int i = 0; i < 60; ++i) {
        while ((int)(T + P.dly[11] - (unsigned)__builtin_amdgcn_s_memrealtime()) > 0) __builtin_amdgcn_s_sleep(1);
        const unsigned ts = OFF_TS + (i + 1) * 16384 + ((hm >> 9) & 1) * 8192 + id * 128;
        unsigned v;
        for (;;) {
            FRESH();
            v = RFL(ld32<SC1>(rws, ts, 0));
            if (v != ~0u || --budget < 0) break;
            __builtin_amdgcn_s_sleep(12);
        }
        asm volatile("" ::: "memory");
        const unsigned T0 = T;
        T += min((int)(v - T), 0x400);
        const unsigned ro = OFF_U + (h2 ^ (((i + 4) & 4) << 11)) + id * 16384 + tid * 16, ra = ((i + 4) & RUA) * 524288, rb = 32 * MB + ((i + 4) & RUB) * 524288;
        if (i + 4 > RUA) {
#pragma nounroll
            while (outp + RUA < (unsigned)i + 4 && --budget >= 0) {
                FRESH();
                const unsigned o = ld32<SC1>(rws, C_OF + lane * 4, 0);
                if (__all(o + RUA >= (unsigned)i + 4)) outp = __all(o + RUA >= (unsigned)i + 20) ? i + 20 - RUA : i + 4 - RUA;
            }
            asm volatile("" ::: "memory");
        }
        st128<SC1>(sent, rws, ro, ra); st128<SC1>(sent, rws, ro, ra + 4096); st128<SC1>(sent, rws, ro, rb); st128<SC1>(sent, rws, ro, rb + 4096);
        reset_piece(rws, i + 6, id, tid, hm);
        while ((int)(T0 + P.dly[4] - (unsigned)__builtin_amdgcn_s_memrealtime()) > 0) {}
        fetch(i);
    }
    WAIT_VM0();
    if (budget < 0 && lane == 0) st32<SC1>(7, rws, C_ERR, 0);
}

__global__ void __launch_bounds__(256, 1) mega_kernel(Params P) {
    extern __shared__ __attribute__((aligned(16))) char lds[];
    const unsigned tk = (unsigned)__builtin_amdgcn_s_memrealtime();
    const rsrc_t rws = mkrs(P.ws, WS_END);
    const int lane = threadIdx.x & 63;
    const int w = __builtin_amdgcn_readfirstlane(threadIdx.x >> 6);
    const unsigned vb = blockIdx.x, xcd = vb & 7, slot = vb >> 3;
    const u32x4 srd = mksrd(P.ws, WS_END);
    if (xcd < 4) {
        const int id = xcd * 8 + (slot & 7);
        if (slot < 8) { if (P.rolemask & 1) role_qk(P, rws, srd, lds, id, w, lane); }
        else if (slot < 16) { if (P.rolemask & 1) role_vo(P, rws, srd, lds, id, w, lane); }
        else if (slot < 24) { if (P.rolemask & 1) role_ow(P, rws, srd, lds, id, w, lane); }
        else { if (P.rolemask & 3) role_cl(P, rws, srd, lds, id, w, lane); }
    } else {
        const int id = (xcd - 4) * 8 + (slot & 7);
        if (slot < 8) { if (P.rolemask & 4) role_kq(P, rws, srd, id, w, lane); }
        else if (slot < 16) { if (P.rolemask & 16) role_vw(P, rws, srd, id, w, lane); }
        else {
            int budget = SPIN_CAP;
            const u32x2 tag = ld64<SC1>(rws, C_EPOCH, 0);
            if (P.rolemask & 2) role_ffn(tk, P, rws, lds, (slot - 16) * 4 + (xcd - 4), w, lane, tag, budget);
            if (P.rolemask & 8) role_out(tk, P, rws, lds, (xcd - 4) * 16 + (slot - 16), w, lane, tag, budget);
        }
    }
}

extern "C" void kernel_launch(void* const* d_in, const int* in_sizes, int n_in, void* d_out, int out_size, void* d_ws, size_t ws_size,
                              hipStream_t stream) {
    Params P;
    P.x = d_in[0]; P.mem0 = d_in[1]; P.wq = d_in[2]; P.wk = d_in[3]; P.wv = d_in[4]; P.wo = d_in[5];
    P.w1 = d_in[8]; P.w2 = d_in[9];
    P.out = d_out;
    P.ws = (unsigned char*)(((uintptr_t)d_ws + (2 * MB - 1)) & ~(uintptr_t)(2 * MB - 1));
    P.rolemask = 543;
    P.dly[0] = DLY_QK_M; P.dly[1] = DLY_QK_K; P.dly[2] = DLY_VO_U; P.dly[3] = DLY_D1; P.dly[13] = DLY_D2; P.dly[14] = DLY_DB; P.dly[15] = DLY_E1; P.dly[16] = DLY_E2; P.dly[4] = DLY_VO_M; P.dly[5] = DLY_MP; P.dly[6] = DLY_PP; P.dly[7] = DLY_X; P.dly[8] = DLY_Q2; P.dly[9] = DLY_FFN; P.dly[10] = DLY_PRE; P.dly[11] = DLY_PRS; P.dly[12] = DLY_PACE; P.dly[17] = DLY_VP; P.dly[18] = DLY_CL;
    P.dly[19] = DLY_S0; P.dly[20] = DLY_S1; P.dly[21] = DLY_S2; P.dly[22] = DLY_S3;
    (void)hipFuncSetAttribute((const void*)mega_kernel, hipFuncAttributeMaxDynamicSharedMemorySize, LDS_BYTES);
    mega_kernel<<<256, 256, LDS_BYTES, stream>>>(P);
}
```
